```python
import torch

def _softmax(x, axis=-1):
    return torch.softmax(x, dim=axis)
def _gelu(x):
    return torch.nn.functional.gelu(x, approximate="tanh")
def _silu(x):
    return torch.nn.functional.silu(x)
def _sigmoid(x):
    return torch.sigmoid(x)
def _layernorm(x, axis=-1, eps=1e-5):
    m = x.mean(dim=axis, keepdim=True)
    v = x.var(dim=axis, keepdim=True, unbiased=False)
    return (x - m) / torch.sqrt(v + eps)
def _scatter_add(tgt, idx, src, axis=0):
    return tgt.clone().index_add_(axis, idx, src)
def _segment_sum(src, seg, n):
    out = torch.zeros((n,) + src.shape[1:], dtype=src.dtype)
    return out.index_add_(0, seg, src)
def _pad(x, pw):
    flat = tuple(v for pair in reversed(pw) for v in pair)
    return torch.nn.functional.pad(x, flat)

kernel_name = 'sequential_preLN__softmax_full_mha__geglu_ffn__prompt'

def setup_inputs():

    _w = (lambda a: (lambda t: t.to(torch.bfloat16) if t.is_floating_point() else t)(torch.as_tensor(a)))
    def _A_setup():
        B, S, D = 1, 2048, 448
        return {
            "x": _w((lambda *s: torch.randn(*s, dtype=torch.bfloat16) * 0.05)(B, S, D)),
            "wqkv": _w((lambda *s: torch.randn(*s, dtype=torch.bfloat16) * 0.05)(D, 3 * D)),
            "wo": _w((lambda *s: torch.randn(*s, dtype=torch.bfloat16) * 0.05)(D, D)),
        }
    _A_in = _A_setup()

    def _B_setup():
        B, S, D, F = 1, 2048, 448, 1792
        return {
            "x": _w((lambda *s: torch.randn(*s, dtype=torch.bfloat16) * 0.05)(B, S, D)),
            "w_gate": _w((lambda *s: torch.randn(*s, dtype=torch.bfloat16) * 0.05)(D, F)),
            "w_up": _w((lambda *s: torch.randn(*s, dtype=torch.bfloat16) * 0.05)(D, F)),
            "w_down": _w((lambda *s: torch.randn(*s, dtype=torch.bfloat16) * 0.05)(F, D)),
        }
    _B_in = _B_setup()

    return {
        "A_x": _A_in["x"],
        "A_wqkv": _A_in["wqkv"],
        "A_wo": _A_in["wo"],
        "B_x": _B_in["x"],
        "B_w_gate": _B_in["w_gate"],
        "B_w_up": _B_in["w_up"],
        "B_w_down": _B_in["w_down"],
    }

def reference(A_x, A_wqkv, A_wo, B_x, B_w_gate, B_w_up, B_w_down):
    def _A_ref(x, wqkv, wo):
        B, S, D = x.shape
        H = 16; Dh = D // H
        qkv = (lambda x, *d: x.permute(*d))((x @ wqkv).reshape(B, S, 3, H, Dh), 2, 0, 3, 1, 4)
        q, k, v = qkv[0], qkv[1], qkv[2]
        scale = Dh ** -0.5
        scores = torch.einsum("bhqd,bhkd->bhqk", q, k) * scale
        p = _softmax(scores, axis=-1)
        o = torch.einsum("bhqk,bhkd->bhqd", p, v)
        return (lambda x, *d: x.permute(*d))(o, 0, 2, 1, 3).reshape(B, S, D) @ wo

    def _B_ref(x, w_gate, w_up, w_down):
        return (_gelu(x @ w_gate) * (x @ w_up)) @ w_down

    x = A_x
    h = x + _A_ref(_layernorm(x, axis=-1), A_wqkv, A_wo)
    h2 = h + _B_ref(_layernorm(h, axis=-1), B_w_gate, B_w_up, B_w_down)
    return h2
```

```cpp
#include <hip/hip_runtime.h>
#include <hip/hip_bf16.h>
#include <stdint.h>

typedef short bf16x8 __attribute__((ext_vector_type(8)));
typedef short s16x4 __attribute__((ext_vector_type(4)));
typedef float f32x16 __attribute__((ext_vector_type(16)));
typedef float f32x4 __attribute__((ext_vector_type(4)));
typedef float f32x2 __attribute__((ext_vector_type(2)));
typedef unsigned u32x4 __attribute__((ext_vector_type(4)));
typedef unsigned u32x2 __attribute__((ext_vector_type(2)));
typedef __bf16 b16x2 __attribute__((ext_vector_type(2)));
#define DI __device__ __forceinline__
#define MFMA(a, b, c) __builtin_amdgcn_mfma_f32_32x32x16_bf16((a), (b), (c), 0, 0, 0)
#define LDS_AS __attribute__((address_space(3)))
#define WAIT_VM(n) asm volatile("s_waitcnt vmcnt(" #n ")" ::: "memory")
#define WAIT_LGKM0() asm volatile("s_waitcnt lgkmcnt(0)" ::: "memory")
#define BAR() __builtin_amdgcn_s_barrier()

constexpr int S_ = 2048, D_ = 448, H_ = 16, F_ = 1792;
constexpr float LN_EPS = 1e-5f;
constexpr float QSCALE = 0.18898223650461362f * 1.4426950408889634f;

constexpr size_t WS_QF = 0;
constexpr size_t WS_KVF = WS_QF + (size_t)16 * 64 * 2048;
constexpr size_t WS_OF = WS_KVF + (size_t)16 * 64 * 4096;
constexpr size_t WS_HF = WS_OF + (size_t)64 * 28 * 1024;
constexpr size_t WS_AF = WS_HF + (size_t)64 * 28 * 1024;
constexpr size_t WS_WOF = WS_AF + (size_t)64 * 112 * 1024;
constexpr size_t WS_WGF = WS_WOF + (size_t)14 * 28 * 1024;
constexpr size_t WS_WDF = WS_WGF + (size_t)112 * 28 * 1024;
constexpr size_t WS_ST = WS_WDF + (size_t)14 * 112 * 1024;
constexpr size_t WS_CS = WS_ST + (size_t)14 * 2048 * 8;
constexpr size_t WS_END = WS_CS + 112 * 32 * 4 + 1024;

DI unsigned pk2(float a, float b) {
  b16x2 v = {(__bf16)a, (__bf16)b};
  return __builtin_bit_cast(unsigned, v);
}
DI float bf_lo(unsigned u) { return __uint_as_float(u << 16); }
DI float bf_hi(unsigned u) { return __uint_as_float(u & 0xffff0000u); }
DI float rbf(float a) { return (float)(__bf16)a; }
DI float dot2(unsigned a, unsigned b, float c) {
  return __builtin_amdgcn_fdot2_f32_bf16(__builtin_bit_cast(b16x2, a), __builtin_bit_cast(b16x2, b), c, false);
}
DI float xhalf_add(float v) {
  auto r = __builtin_amdgcn_permlane32_swap(__float_as_uint(v), __float_as_uint(v), false, false);
  return __uint_as_float(r[0]) + __uint_as_float(r[1]);
}
DI float xhalf_max(float v) {
  auto r = __builtin_amdgcn_permlane32_swap(__float_as_uint(v), __float_as_uint(v), false, false);
  return fmaxf(__uint_as_float(r[0]), __uint_as_float(r[1]));
}
DI bf16x8 pack8(const f32x16& a, int s) {
  u32x4 w = {pk2(a[8 * s], a[8 * s + 1]), pk2(a[8 * s + 2], a[8 * s + 3]), pk2(a[8 * s + 4], a[8 * s + 5]),
             pk2(a[8 * s + 6], a[8 * s + 7])};
  return __builtin_bit_cast(bf16x8, w);
}
DI void glds16(const void* g, void* l) {
  __builtin_amdgcn_global_load_lds((const __attribute__((address_space(1))) unsigned*)g, (LDS_AS unsigned*)l, 16, 0, 0);
}
DI s16x4 tr_read(const char* p) { return __builtin_amdgcn_ds_read_tr16_b64_v4i16((LDS_AS s16x4*)p); }
DI u32x2 neg_split3(float m) {
  float n = -m;
  float a = rbf(n), r1 = n - a, b = rbf(r1), r2 = r1 - b;
  return u32x2{pk2(a, b), pk2(r2, 0.f)};
}
constexpr unsigned ONES2 = 0x3f803f80u;

constexpr int K1_WIMG = 448 * 64;
constexpr int K1_XOFF = 3 * K1_WIMG;
constexpr int K1_LDS = K1_XOFF + 4 * 4 * 4096;

__global__ __launch_bounds__(256, 1) void k1_qkv(const uint16_t* __restrict__ x, const uint16_t* __restrict__ wqkv,
                                                 char* __restrict__ ws) {
  extern __shared__ __attribute__((aligned(16))) char sm[];
  const int tid = threadIdx.x, lane = tid & 63, w = __builtin_amdgcn_readfirstlane(tid >> 6);
  const int b = blockIdx.x, xcd = b & 7, idx = b >> 3;
  const int tt = (xcd >> 1) * 4 + (idx & 3), h = (xcd & 1) * 8 + (idx >> 2);
  const int r = lane & 31, hh = lane >> 5;
  const char* wsrc = (const char*)wqkv + (size_t)(16 * w + (lane >> 2)) * 2688 + ((56 * h) & ~15) + (lane & 3) * 16;
  const char* xsrc0 = (const char*)x + (size_t)(tt * 128 + w * 32 + (lane >> 3)) * 896 + (((lane & 7) ^ (lane >> 4)) * 16);
  const char* xsrc1 = (const char*)x + (size_t)(tt * 128 + w * 32 + 8 + (lane >> 3)) * 896 + (((lane & 7) ^ (4 + (lane >> 4))) * 16);
  char* xl = sm + K1_XOFF + w * 16384;
#define K1_LDW(kc)                                                                                          \
  _Pragma("unroll") for (int s = 0; s < 3; ++s)                                                             \
      glds16(wsrc + (size_t)(kc) * 64 * 2688 + s * 896, sm + s * K1_WIMG + ((kc) * 64 + 16 * w) * 64);
#define K1_LDX(kc)                                                                                          \
  _Pragma("unroll") for (int t = 0; t < 2; ++t) {                                                           \
    glds16(xsrc0 + (size_t)t * 16 * 896 + (kc) * 128, xl + ((kc) & 3) * 4096 + t * 2048);                   \
    glds16(xsrc1 + (size_t)t * 16 * 896 + (kc) * 128, xl + ((kc) & 3) * 4096 + t * 2048 + 1024);            \
  }
  K1_LDW(0) K1_LDX(0) K1_LDW(1) K1_LDX(1) K1_LDW(2) K1_LDX(2) K1_LDW(3) K1_LDX(3) K1_LDW(4) K1_LDW(5) K1_LDW(6)

  const char* xrd = xl + r * 128;
  const int xsw = (r >> 1) & 7;
  const char* wrd = sm + (8 * hh + ((lane & 15) >> 2)) * 64 + 8 * (h & 1) + 32 * ((lane >> 4) & 1) + 8 * (lane & 3);
  f32x16 aq = {}, ak = {}, av = {};
  float sx = 0.f, sxx = 0.f, csq = 0.f, csk = 0.f, csv = 0.f;
#pragma unroll
  for (int kc = 0; kc < 7; ++kc) {
    if (kc == 0) WAIT_VM(30);
    else if (kc == 1) WAIT_VM(27);
    else if (kc == 2) WAIT_VM(24);
    else if (kc == 3) WAIT_VM(21);
    else if (kc == 4) WAIT_VM(8);
    else if (kc == 5) WAIT_VM(4);
    else WAIT_VM(0);
    if (kc < 5) BAR();
    asm volatile("" ::: "memory");
#pragma unroll
    for (int s = 0; s < 4; ++s) {
      const int ks = kc * 4 + s;
      const bf16x8 xf = *(const bf16x8*)(xrd + (kc & 3) * 4096 + (((2 * s + hh) ^ xsw) * 16));
      bf16x8 wf[3];
#pragma unroll
      for (int m = 0; m < 3; ++m) {
        s16x4 lo = tr_read(wrd + m * K1_WIMG + ks * 1024), hi = tr_read(wrd + m * K1_WIMG + ks * 1024 + 256);
        wf[m] = __builtin_shufflevector(lo, hi, 0, 1, 2, 3, 4, 5, 6, 7);
      }
      aq = MFMA(wf[0], xf, aq);
      ak = MFMA(wf[1], xf, ak);
      av = MFMA(xf, wf[2], av);
      const u32x4 xu = __builtin_bit_cast(u32x4, xf);
#pragma unroll
      for (int j = 0; j < 4; ++j) { sx = dot2(xu[j], ONES2, sx); sxx = dot2(xu[j], xu[j], sxx); }
      if (s == w) {
        const u32x4 u0 = __builtin_bit_cast(u32x4, wf[0]), u1 = __builtin_bit_cast(u32x4, wf[1]), u2 = __builtin_bit_cast(u32x4, wf[2]);
#pragma unroll
        for (int j = 0; j < 4; ++j) { csq = dot2(u0[j], ONES2, csq); csk = dot2(u1[j], ONES2, csk); csv = dot2(u2[j], ONES2, csv); }
      }
    }
    if (kc < 3) {
      WAIT_LGKM0();
      if (kc == 0) { K1_LDX(4) } else if (kc == 1) { K1_LDX(5) } else { K1_LDX(6) }
    }
  }
  sx = xhalf_add(sx); sxx = xhalf_add(sxx);
  csq = xhalf_add(csq); csk = xhalf_add(csk); csv = xhalf_add(csv);
  const float mean = sx * (1.f / 448.f);
  const float var = fmaxf(sxx * (1.f / 448.f) - mean * mean, 0.f);
  const float rstd = __builtin_amdgcn_rsqf(var + LN_EPS);
  float* tb = (float*)xl;
  WAIT_LGKM0();
  if (hh == 0) { tb[r] = csq; tb[32 + r] = csk; tb[64 + r] = csv; tb[96 + r] = mean; tb[128 + r] = rstd; }
  __syncthreads();
  const float* t0 = (const float*)(sm + K1_XOFF);
  const size_t tile = (size_t)h * 64 + tt * 4 + w;
  float cv = 0.f;
#pragma unroll
  for (int ww = 0; ww < 4; ++ww) cv += t0[ww * 4096 + 64 + r];
  float sii = 0.f;
  u32x4 qo[2], ko[2], vo[2];
#pragma unroll
  for (int g = 0; g < 4; ++g) {
    f32x4 cq = {}, ck = {};
#pragma unroll
    for (int ww = 0; ww < 4; ++ww) {
      cq += *(const f32x4*)(t0 + ww * 4096 + 8 * g + 4 * hh);
      ck += *(const f32x4*)(t0 + ww * 4096 + 32 + 8 * g + 4 * hh);
    }
    const f32x4 mu = *(const f32x4*)(tb + 96 + 8 * g + 4 * hh), rs = *(const f32x4*)(tb + 128 + 8 * g + 4 * hh);
    float q[4], k[4], v[4];
#pragma unroll
    for (int j = 0; j < 4; ++j) {
      q[j] = (aq[4 * g + j] - mean * cq[j]) * (rstd * QSCALE);
      k[j] = (ak[4 * g + j] - mean * ck[j]) * rstd;
      v[j] = (av[4 * g + j] - mu[j] * cv) * rs[j];
      if (r >= 28) v[j] = (r == 28) ? 1.f : 0.f;
    }
    unsigned q0 = pk2(q[0], q[1]), q1 = pk2(q[2], q[3]), k0 = pk2(k[0], k[1]), k1 = pk2(k[2], k[3]);
    if (g == 3 && hh == 1) { q0 = 0; q1 = 0; k0 = ONES2; k1 = 0x00003f80u; }
    sii = dot2(q0, k0, sii); sii = dot2(q1, k1, sii);
    qo[g >> 1][2 * (g & 1)] = q0; qo[g >> 1][2 * (g & 1) + 1] = q1;
    ko[g >> 1][2 * (g & 1)] = k0; ko[g >> 1][2 * (g & 1) + 1] = k1;
    vo[g >> 1][2 * (g & 1)] = pk2(v[0], v[1]); vo[g >> 1][2 * (g & 1) + 1] = pk2(v[2], v[3]);
  }
  sii = xhalf_add(sii);
  if (hh == 1) { u32x2 ms = neg_split3(sii); qo[1][2] = ms[0]; qo[1][3] = ms[1]; }
  char* qd = ws + WS_QF + tile * 2048 + lane * 16;
  char* kd = ws + WS_KVF + tile * 4096 + lane * 16;
  *(u32x4*)(qd) = qo[0]; *(u32x4*)(qd + 1024) = qo[1];
  *(u32x4*)(kd) = ko[0]; *(u32x4*)(kd + 1024) = ko[1];
  *(u32x4*)(kd + 2048) = vo[0]; *(u32x4*)(kd + 3072) = vo[1];
}

constexpr int K2_LDS = 8 * 16384 + 8 * 512 + 64;

__global__ __launch_bounds__(512, 2) void k2_attn(const uint16_t* __restrict__ wo, const uint16_t* __restrict__ wg,
                                                  const uint16_t* __restrict__ wu, const uint16_t* __restrict__ wd,
                                                  char* __restrict__ ws) {
  extern __shared__ __attribute__((aligned(16))) char sm[];
  const int tid = threadIdx.x, lane = tid & 63, w = __builtin_amdgcn_readfirstlane(tid >> 6);
  const int b = blockIdx.x, xcd = b & 7, idx = b >> 3;
  const int h = 2 * xcd + (idx & 1), qt = idx >> 1;
  const int r = lane & 31, hh = lane >> 5;
  const char* qsrc = ws + WS_QF + ((size_t)h * 64 + qt * 4) * 2048 + lane * 16;
  const char* kvsrc = ws + WS_KVF + ((size_t)h * 64 + w * 8) * 4096 + lane * 16;
  bf16x8 q[4][2];
#pragma unroll
  for (int i = 0; i < 4; ++i) { q[i][0] = *(const bf16x8*)(qsrc + i * 2048); q[i][1] = *(const bf16x8*)(qsrc + i * 2048 + 1024); }
  const int oi = w >> 1, og = 2 * (w & 1);
  f32x4 res[2];
  int* flag = (int*)(sm + 8 * 16384 + 8 * 512);
  if (tid == 0) *flag = 0;
#pragma unroll 1
  for (int pass = 0;; ++pass) {
    f32x16 o[4] = {};
    bf16x8 kv[3][4];
#pragma unroll
    for (int t = 0; t < 2; ++t)
#pragma unroll
      for (int c = 0; c < 4; ++c) kv[t][c] = *(const bf16x8*)(kvsrc + t * 4096 + c * 1024);
#pragma unroll
    for (int t = 0; t < 8; ++t) {
      if (t + 2 < 8) {
#pragma unroll
        for (int c = 0; c < 4; ++c) kv[(t + 2) % 3][c] = *(const bf16x8*)(kvsrc + (t + 2) * 4096 + c * 1024);
      }
#pragma unroll
      for (int i = 0; i < 4; ++i) {
        f32x16 s = {};
        s = MFMA(kv[t % 3][0], q[i][0], s);
        s = MFMA(kv[t % 3][1], q[i][1], s);
#pragma unroll
        for (int e = 0; e < 16; ++e) s[e] = __builtin_amdgcn_exp2f(s[e]);
        o[i] = MFMA(kv[t % 3][2], pack8(s, 0), o[i]);
        o[i] = MFMA(kv[t % 3][3], pack8(s, 1), o[i]);
      }
    }
#pragma unroll
    for (int i = 0; i < 4; ++i)
#pragma unroll
      for (int g = 0; g < 4; ++g)
        *(f32x4*)(sm + w * 16384 + (i * 4 + g) * 1024 + lane * 16) = f32x4{o[i][4 * g], o[i][4 * g + 1], o[i][4 * g + 2], o[i][4 * g + 3]};
    __syncthreads();
    float l = 0.f;
    res[0] = f32x4{}; res[1] = f32x4{};
#pragma unroll
    for (int ww = 0; ww < 8; ++ww) {
      res[0] += *(const f32x4*)(sm + ww * 16384 + (oi * 4 + og) * 1024 + lane * 16);
      res[1] += *(const f32x4*)(sm + ww * 16384 + (oi * 4 + og + 1) * 1024 + lane * 16);
      l += *(const float*)(sm + ww * 16384 + (oi * 4 + 3) * 1024 + (32 + r) * 16);
    }
    const float rl = 1.f / l;
    res[0] *= rl; res[1] *= rl;
    if (pass == 1) break;
    float chk = l;
#pragma unroll
    for (int j = 0; j < 4; ++j) chk += fabsf(res[0][j]) * 0.f + fabsf(res[1][j]) * 0.f;
    const bool bad = !(chk < 1.0e18f);
    if (__any(bad)) { if (lane == 0) atomicOr(flag, 1); }
    __syncthreads();
    if (*(volatile int*)flag == 0) break;
    float* mx = (float*)(sm + 8 * 16384);
#pragma unroll
    for (int i = 0; i < 4; ++i) {
      u32x4 qq = __builtin_bit_cast(u32x4, q[i][1]);
      if (hh == 1) { qq[2] = 0; qq[3] = 0; }
      q[i][1] = __builtin_bit_cast(bf16x8, qq);
    }
    float m[4] = {-INFINITY, -INFINITY, -INFINITY, -INFINITY};
#pragma unroll 1
    for (int t = 0; t < 8; ++t) {
      const bf16x8 k0 = *(const bf16x8*)(kvsrc + t * 4096), k1 = *(const bf16x8*)(kvsrc + t * 4096 + 1024);
#pragma unroll
      for (int i = 0; i < 4; ++i) {
        f32x16 s = {};
        s = MFMA(k0, q[i][0], s);
        s = MFMA(k1, q[i][1], s);
#pragma unroll
        for (int e = 0; e < 16; ++e) m[i] = fmaxf(m[i], s[e]);
      }
    }
#pragma unroll
    for (int i = 0; i < 4; ++i) { m[i] = xhalf_max(m[i]); if (hh == 0) mx[w * 128 + i * 32 + r] = m[i]; }
    __syncthreads();
#pragma unroll
    for (int i = 0; i < 4; ++i) {
      float mm = -INFINITY;
#pragma unroll
      for (int ww = 0; ww < 8; ++ww) mm = fmaxf(mm, mx[ww * 128 + i * 32 + r]);
      u32x4 qq = __builtin_bit_cast(u32x4, q[i][1]);
      const u32x2 ms = neg_split3(mm);
      if (hh == 1) { qq[2] = ms[0]; qq[3] = ms[1]; }
      q[i][1] = __builtin_bit_cast(bf16x8, qq);
    }
    __syncthreads();
  }
#pragma unroll
  for (int c = 0; c < 2; ++c) {
    const int gl = 2 * (og + c) + hh;
    const int gi = 7 * h + gl;
    if (gl < 7) {
      char* od = ws + WS_OF + ((size_t)(qt * 4 + oi) * 28 + (gi >> 2)) * 1024 + ((gi & 1) * 32 + r) * 16 + ((gi >> 1) & 1) * 8;
      *(u32x2*)od = u32x2{pk2(res[c][0], res[c][1]), pk2(res[c][2], res[c][3])};
    }
  }
  const int gw = b * 8 + w;
#pragma unroll
  for (int it = 0; it < 3; ++it) {
    const int f = gw + it * 2048;
    if (f < 5096) {
      const uint16_t* src; int stride; char* dst;
      if (f < 3136) {
        const int cl = f & 3, t = f >> 2, ks = t % 28, c = (t / 28) * 4 + cl;
        src = (r < 16 ? wg : wu) + (size_t)(16 * ks + 4 * hh) * 1792 + 16 * c + (r & 15); stride = 1792;
        dst = ws + WS_WGF + ((size_t)c * 28 + ks) * 1024;
      } else if (f < 3136 + 1568) {
        const int g = f - 3136, nl = g & 1, t = g >> 1, ks = t % 112, nb = (t / 112) * 2 + nl;
        src = wd + (size_t)(16 * ks + 4 * hh) * 448 + 32 * nb + r; stride = 448;
        dst = ws + WS_WDF + ((size_t)nb * 112 + ks) * 1024;
      } else {
        const int g = f - 4704, nl = g & 1, t = g >> 1, ks = t % 28, nb = (t / 28) * 2 + nl;
        src = wo + (size_t)(16 * ks + 4 * hh) * 448 + 32 * nb + r; stride = 448;
        dst = ws + WS_WOF + ((size_t)nb * 28 + ks) * 1024;
      }
      unsigned e[8];
#pragma unroll
      for (int j = 0; j < 8; ++j) e[j] = src[(size_t)(8 * (j >> 2) + (j & 3)) * stride];
      *(u32x4*)(dst + lane * 16) = u32x4{e[0] | (e[1] << 16), e[2] | (e[3] << 16), e[4] | (e[5] << 16), e[6] | (e[7] << 16)};
    }
  }
}

DI f32x16 reduce4(f32x16 (&acc)[2][2], char* sm, int w, int lane) {
#pragma unroll
  for (int t = 0; t < 4; ++t)
#pragma unroll
    for (int g = 0; g < 4; ++g) {
      const f32x16& a = acc[t >> 1][t & 1];
      *(f32x4*)(sm + ((w * 4 + t) * 4 + g) * 1024 + lane * 16) = f32x4{a[4 * g], a[4 * g + 1], a[4 * g + 2], a[4 * g + 3]};
    }
  __syncthreads();
  f32x16 o;
#pragma unroll
  for (int g = 0; g < 4; ++g) {
    f32x4 s = *(const f32x4*)(sm + ((0 * 4 + w) * 4 + g) * 1024 + lane * 16);
#pragma unroll
    for (int ww = 1; ww < 4; ++ww) s += *(const f32x4*)(sm + ((ww * 4 + w) * 4 + g) * 1024 + lane * 16);
    o[4 * g] = s[0]; o[4 * g + 1] = s[1]; o[4 * g + 2] = s[2]; o[4 * g + 3] = s[3];
  }
  return o;
}

__global__ __launch_bounds__(256, 1) void k3_oproj(const uint16_t* __restrict__ x, char* __restrict__ ws) {
  extern __shared__ __attribute__((aligned(16))) char sm[];
  const int tid = threadIdx.x, lane = tid & 63, w = __builtin_amdgcn_readfirstlane(tid >> 6);
  const int b = blockIdx.x, r = lane & 31, hh = lane >> 5;
  if (b >= 224) {
    const int c = (b - 224) * 4 + w;
    const char* src = ws + WS_WGF + (size_t)c * 28 * 1024 + lane * 16;
    u32x4 v[28];
#pragma unroll
    for (int i = 0; i < 28; ++i) v[i] = *(const u32x4*)(src + i * 1024);
    float s0 = 0.f, s1 = 0.f;
#pragma unroll
    for (int i = 0; i < 28; ++i) { s0 = dot2(v[i][0], ONES2, s0); s1 = dot2(v[i][1], ONES2, s1); s0 = dot2(v[i][2], ONES2, s0); s1 = dot2(v[i][3], ONES2, s1); }
    const float s = xhalf_add(s0 + s1);
    if (hh == 0) ((float*)(ws + WS_CS))[c * 32 + r] = s;
    return;
  }
  const int xcd = b & 7, idx = b >> 3, t64 = 4 * xcd + (idx & 3), nb = idx >> 2;
  const char* osrc = ws + WS_OF + ((size_t)(2 * t64) * 28 + 7 * w) * 1024 + lane * 16;
  const char* wsrc = ws + WS_WOF + ((size_t)(2 * nb) * 28 + 7 * w) * 1024 + lane * 16;
  bf16x8 of[7][2], wf[7][2];
#pragma unroll
  for (int s = 0; s < 7; ++s) {
    of[s][0] = *(const bf16x8*)(osrc + s * 1024); wf[s][0] = *(const bf16x8*)(wsrc + s * 1024);
    of[s][1] = *(const bf16x8*)(osrc + (28 + s) * 1024); wf[s][1] = *(const bf16x8*)(wsrc + (28 + s) * 1024);
  }
  const int tt32 = 2 * t64 + (w & 1), n32 = 2 * nb + (w >> 1);
  const uint16_t* xs = x + (size_t)(tt32 * 32 + r) * 448 + n32 * 32 + 4 * hh;
  u32x2 xr[4];
#pragma unroll
  for (int g = 0; g < 4; ++g) xr[g] = *(const u32x2*)(xs + 8 * g);
  f32x16 acc[2][2] = {};
#pragma unroll
  for (int s = 0; s < 7; ++s)
#pragma unroll
    for (int c = 0; c < 2; ++c)
#pragma unroll
      for (int a = 0; a < 2; ++a) acc[c][a] = MFMA(wf[s][c], of[s][a], acc[c][a]);
  f32x16 o = reduce4(acc, sm, w, lane);
  float s1 = 0.f, s2 = 0.f;
  u32x4 ho[2];
#pragma unroll
  for (int g = 0; g < 4; ++g) {
    const float h0 = bf_lo(xr[g][0]) + rbf(o[4 * g]), h1 = bf_hi(xr[g][0]) + rbf(o[4 * g + 1]);
    const float h2 = bf_lo(xr[g][1]) + rbf(o[4 * g + 2]), h3 = bf_hi(xr[g][1]) + rbf(o[4 * g + 3]);
    const unsigned p0 = pk2(h0, h1), p1 = pk2(h2, h3);
    s1 = dot2(p0, ONES2, s1); s1 = dot2(p1, ONES2, s1);
    s2 = dot2(p0, p0, s2); s2 = dot2(p1, p1, s2);
    ho[g >> 1][2 * (g & 1)] = p0; ho[g >> 1][2 * (g & 1) + 1] = p1;
  }
  char* hd = ws + WS_HF + ((size_t)tt32 * 28 + 2 * n32) * 1024 + lane * 16;
  *(u32x4*)hd = ho[0]; *(u32x4*)(hd + 1024) = ho[1];
  s1 = xhalf_add(s1); s2 = xhalf_add(s2);
  if (hh == 0) *(f32x2*)(ws + WS_ST + ((size_t)n32 * 2048 + tt32 * 32 + r) * 8) = f32x2{s1, s2};
}

constexpr int K4_CH = 28 * 1024;
constexpr int K4_ST = 5 * K4_CH;
constexpr int K4_CS = K4_ST + 14 * 1024;
constexpr int K4_LDS = K4_CS + 1024;

__global__ __launch_bounds__(256, 1) void k4_ffn_up(char* __restrict__ ws) {
  extern __shared__ __attribute__((aligned(16))) char sm[];
  const int tid = threadIdx.x, lane = tid & 63, w = __builtin_amdgcn_readfirstlane(tid >> 6);
  const int b = blockIdx.x, xcd = b & 7, idx = b >> 3;
  const int tt = (xcd & 1) * 8 + (idx & 7), cg = (xcd >> 1) * 4 + (idx >> 3);
  const int r = lane & 31, hh = lane >> 5;
  const char* wsrc = ws + WS_WGF + (size_t)cg * 7 * K4_CH + w * 7168 + lane * 16;
  const char* hsrc = ws + WS_HF + ((size_t)(tt * 4 + w) * 28) * 1024 + lane * 16;
#define K4_LDW(c) _Pragma("unroll") for (int i = 0; i < 7; ++i) glds16(wsrc + (c) * K4_CH + i * 1024, sm + ((c) % 5) * K4_CH + w * 7168 + i * 1024);
  K4_LDW(0)
  bf16x8 hf[28];
#pragma unroll
  for (int i = 0; i < 28; ++i)
    asm volatile("global_load_dwordx4 %0, %1, off" : "=v"(hf[i]) : "v"(hsrc + i * 1024) : "memory");
  {
    const char* st = ws + WS_ST + (size_t)tt * 1024 + lane * 16;
#pragma unroll
    for (int i = 0; i < 4; ++i) {
      const int n = w * 4 + i;
      const char* src = n < 14 ? st + (size_t)n * 16384 : ws + WS_CS + cg * 896 + lane * 16;
      glds16(src, sm + K4_ST + (n < 14 ? n : 14) * 1024);
    }
  }
  K4_LDW(1) K4_LDW(2) K4_LDW(3)
  asm volatile("s_waitcnt vmcnt(21)"
               : "+v"(hf[0]), "+v"(hf[1]), "+v"(hf[2]), "+v"(hf[3]), "+v"(hf[4]), "+v"(hf[5]), "+v"(hf[6]), "+v"(hf[7]),
                 "+v"(hf[8]), "+v"(hf[9]), "+v"(hf[10]), "+v"(hf[11]), "+v"(hf[12]), "+v"(hf[13])
               :: "memory");
  asm volatile(""
               : "+v"(hf[14]), "+v"(hf[15]), "+v"(hf[16]), "+v"(hf[17]), "+v"(hf[18]), "+v"(hf[19]), "+v"(hf[20]), "+v"(hf[21]),
                 "+v"(hf[22]), "+v"(hf[23]), "+v"(hf[24]), "+v"(hf[25]), "+v"(hf[26]), "+v"(hf[27])
               :: "memory");
  BAR();
  asm volatile("" ::: "memory");
  K4_LDW(4)
  float s1 = 0.f, s2 = 0.f;
#pragma unroll
  for (int i = 0; i < 7; ++i) {
    const f32x2 p = *(const f32x2*)(sm + K4_ST + (hh * 7 + i) * 1024 + (w * 32 + r) * 8);
    s1 += p[0]; s2 += p[1];
  }
  s1 = xhalf_add(s1); s2 = xhalf_add(s2);
  const float mean = s1 * (1.f / 448.f);
  const float rstd = __builtin_amdgcn_rsqf(fmaxf(s2 * (1.f / 448.f) - mean * mean, 0.f) + LN_EPS);
  char* adst = ws + WS_AF + ((size_t)(tt * 4 + w) * 112 + cg * 7) * 1024 + lane * 16;
  constexpr float GC1 = -2.f * 0.7978845608028654f * 1.4426950408889634f, GC3 = GC1 * 0.044715f;
#pragma unroll
  for (int c = 0; c < 7; ++c) {
    if (c == 1) { WAIT_VM(22); BAR(); }
    else if (c == 2) { WAIT_VM(23); BAR(); }
    else if (c == 3) { WAIT_VM(24); BAR(); }
    else if (c == 4) { WAIT_VM(18); BAR(); }
    else if (c == 5) { WAIT_VM(11); BAR(); }
    else if (c == 6) { WAIT_VM(4); BAR(); }
    asm volatile("" ::: "memory");
    if (c == 1) { K4_LDW(5) } else if (c == 2) { K4_LDW(6) }
    f32x16 acc = {};
#pragma unroll
    for (int ks = 0; ks < 28; ++ks) {
      const bf16x8 wf = *(const bf16x8*)(sm + (c % 5) * K4_CH + ks * 1024 + lane * 16);
      acc = MFMA(wf, hf[ks], acc);
    }
    float a[8];
#pragma unroll
    for (int g = 0; g < 2; ++g) {
      const f32x4 cg_ = *(const f32x4*)(sm + K4_CS + (c * 32 + 8 * g + 4 * hh) * 4);
      const f32x4 cu_ = *(const f32x4*)(sm + K4_CS + (c * 32 + 16 + 8 * g + 4 * hh) * 4);
#pragma unroll
      for (int j = 0; j < 4; ++j) {
        const float gt = (acc[4 * g + j] - mean * cg_[j]) * rstd;
        const float up = (acc[8 + 4 * g + j] - mean * cu_[j]) * rstd;
        const float e = __builtin_amdgcn_exp2f(gt * fmaf(gt * gt, GC3, GC1));
        a[4 * g + j] = gt * __builtin_amdgcn_rcpf(1.f + e) * up;
      }
    }
    *(u32x4*)(adst + c * 1024) = u32x4{pk2(a[0], a[1]), pk2(a[2], a[3]), pk2(a[4], a[5]), pk2(a[6], a[7])};
    WAIT_LGKM0();
  }
}

__global__ __launch_bounds__(256, 1) void k5_ffn_down(const char* __restrict__ ws, uint16_t* __restrict__ out) {
  extern __shared__ __attribute__((aligned(16))) char sm[];
  const int tid = threadIdx.x, lane = tid & 63, w = __builtin_amdgcn_readfirstlane(tid >> 6);
  const int b = blockIdx.x, r = lane & 31, hh = lane >> 5;
  const int xcd = b & 7, idx = b >> 3, t64 = 4 * xcd + (idx & 3), nb = idx >> 2;
  const char* asrc = ws + WS_AF + ((size_t)(2 * t64) * 112 + 28 * w) * 1024 + lane * 16;
  const char* wsrc = ws + WS_WDF + ((size_t)(2 * nb) * 112 + 28 * w) * 1024 + lane * 16;
  constexpr int P = 14;
  bf16x8 af[P][2], wf[P][2];
#pragma unroll
  for (int s = 0; s < P; ++s) {
    af[s][0] = *(const bf16x8*)(asrc + s * 1024); wf[s][0] = *(const bf16x8*)(wsrc + s * 1024);
    af[s][1] = *(const bf16x8*)(asrc + (112 + s) * 1024); wf[s][1] = *(const bf16x8*)(wsrc + (112 + s) * 1024);
  }
  f32x16 acc[2][2] = {};
#pragma unroll
  for (int s = 0; s < 28; ++s) {
#pragma unroll
    for (int c = 0; c < 2; ++c)
#pragma unroll
      for (int a = 0; a < 2; ++a) acc[c][a] = MFMA(wf[s % P][c], af[s % P][a], acc[c][a]);
    if (s + P < 28) {
      af[s % P][0] = *(const bf16x8*)(asrc + (s + P) * 1024); wf[s % P][0] = *(const bf16x8*)(wsrc + (s + P) * 1024);
      af[s % P][1] = *(const bf16x8*)(asrc + (112 + s + P) * 1024); wf[s % P][1] = *(const bf16x8*)(wsrc + (112 + s + P) * 1024);
    }
  }
  const int tt32 = 2 * t64 + (w & 1), n32 = 2 * nb + (w >> 1);
  const char* hs = ws + WS_HF + ((size_t)tt32 * 28 + 2 * n32) * 1024 + lane * 16;
  const u32x4 h0 = *(const u32x4*)hs, h1 = *(const u32x4*)(hs + 1024);
  f32x16 o = reduce4(acc, sm, w, lane);
  unsigned pq[4][2];
#pragma unroll
  for (int g = 0; g < 4; ++g) {
    const unsigned ha = g < 2 ? h0[2 * (g & 1)] : h1[2 * (g & 1)], hb = g < 2 ? h0[2 * (g & 1) + 1] : h1[2 * (g & 1) + 1];
    pq[g][0] = pk2(bf_lo(ha) + rbf(o[4 * g]), bf_hi(ha) + rbf(o[4 * g + 1]));
    pq[g][1] = pk2(bf_lo(hb) + rbf(o[4 * g + 2]), bf_hi(hb) + rbf(o[4 * g + 3]));
  }
  uint16_t* od = out + (size_t)(tt32 * 32 + r) * 448 + n32 * 32 + 8 * hh;
#pragma unroll
  for (int g = 0; g < 4; g += 2) {
    auto r0 = __builtin_amdgcn_permlane32_swap(pq[g][0], pq[g + 1][0], false, false);
    auto r1 = __builtin_amdgcn_permlane32_swap(pq[g][1], pq[g + 1][1], false, false);
    *(u32x4*)(od + 8 * g) = u32x4{r0[0], r1[0], r0[1], r1[1]};
  }
}

extern "C" void kernel_launch(void* const* d_in, const int* in_sizes, int n_in, void* d_out, int out_size, void* d_ws,
                              size_t ws_size, hipStream_t stream) {
  const uint16_t* x = (const uint16_t*)d_in[0];
  const uint16_t* wqkv = (const uint16_t*)d_in[1];
  const uint16_t* wo = (const uint16_t*)d_in[2];
  const uint16_t* wg = (const uint16_t*)d_in[4];
  const uint16_t* wu = (const uint16_t*)d_in[5];
  const uint16_t* wd = (const uint16_t*)d_in[6];
  char* ws = (char*)d_ws;
  (void)hipFuncSetAttribute((const void*)k1_qkv, hipFuncAttributeMaxDynamicSharedMemorySize, K1_LDS);
  (void)hipFuncSetAttribute((const void*)k2_attn, hipFuncAttributeMaxDynamicSharedMemorySize, K2_LDS);
  (void)hipFuncSetAttribute((const void*)k4_ffn_up, hipFuncAttributeMaxDynamicSharedMemorySize, K4_LDS);
  k1_qkv<<<256, 256, K1_LDS, stream>>>(x, wqkv, ws);
  k2_attn<<<256, 512, K2_LDS, stream>>>(wo, wg, wu, wd, ws);
  k3_oproj<<<252, 256, 65536, stream>>>(x, ws);
  k4_ffn_up<<<256, 256, K4_LDS, stream>>>(ws);
  k5_ffn_down<<<224, 256, 65536, stream>>>(ws, (uint16_t*)d_out);
}
```

```cpp
#include <hip/hip_runtime.h>
#include <hip/hip_bf16.h>
#include <stdint.h>

typedef short bf16x8 __attribute__((ext_vector_type(8)));
typedef short s16x4 __attribute__((ext_vector_type(4)));
typedef float f32x16 __attribute__((ext_vector_type(16)));
typedef float f32x4 __attribute__((ext_vector_type(4)));
typedef float f32x2 __attribute__((ext_vector_type(2)));
typedef unsigned u32x4 __attribute__((ext_vector_type(4)));
typedef unsigned u32x2 __attribute__((ext_vector_type(2)));
typedef __bf16 b16x2 __attribute__((ext_vector_type(2)));
#define DI __device__ __forceinline__
#define MFMA(a, b, c) __builtin_amdgcn_mfma_f32_32x32x16_bf16((a), (b), (c), 0, 0, 0)
#define LDS_AS __attribute__((address_space(3)))
#define WAIT_VM(n) asm volatile("s_waitcnt vmcnt(" #n ")" ::: "memory")
#define WAIT_LGKM0() asm volatile("s_waitcnt lgkmcnt(0)" ::: "memory")
#define BAR() __builtin_amdgcn_s_barrier()
#define SB() __builtin_amdgcn_sched_barrier(0)

constexpr int S_ = 2048, D_ = 448, H_ = 16, F_ = 1792;
constexpr float LN_EPS = 1e-5f;
constexpr float QSCALE = 0.18898223650461362f * 1.4426950408889634f;

constexpr size_t WS_QF = 0;
constexpr size_t WS_KVF = WS_QF + (size_t)16 * 64 * 2048;
constexpr size_t WS_OF = WS_KVF + (size_t)16 * 64 * 4096;
constexpr size_t WS_HF = WS_OF + (size_t)64 * 28 * 1024;
constexpr size_t WS_AF = WS_HF + (size_t)64 * 28 * 1024;
constexpr size_t WS_WOF = WS_AF + (size_t)64 * 112 * 1024;
constexpr size_t WS_WGF = WS_WOF + (size_t)14 * 28 * 1024;
constexpr size_t WS_WDF = WS_WGF + (size_t)112 * 28 * 1024;
constexpr size_t WS_ST = WS_WDF + (size_t)14 * 112 * 1024;
constexpr size_t WS_CS = WS_ST + (size_t)14 * 2048 * 8;
constexpr size_t WS_END = WS_CS + 112 * 32 * 4 + 1024;
#define TS_DECL
#define TS(i)
#define TS_OUT(k)
#define CY0()
#define CY1()

DI unsigned pk2(float a, float b) {
  b16x2 v = {(__bf16)a, (__bf16)b};
  return __builtin_bit_cast(unsigned, v);
}
DI float bf_lo(unsigned u) { return __uint_as_float(u << 16); }
DI float bf_hi(unsigned u) { return __uint_as_float(u & 0xffff0000u); }
DI float rbf(float a) { return (float)(__bf16)a; }
DI float dot2(unsigned a, unsigned b, float c) {
  return __builtin_amdgcn_fdot2_f32_bf16(__builtin_bit_cast(b16x2, a), __builtin_bit_cast(b16x2, b), c, false);
}
DI float xhalf_add(float v) {
  auto r = __builtin_amdgcn_permlane32_swap(__float_as_uint(v), __float_as_uint(v), false, false);
  return __uint_as_float(r[0]) + __uint_as_float(r[1]);
}
DI float xhalf_max(float v) {
  auto r = __builtin_amdgcn_permlane32_swap(__float_as_uint(v), __float_as_uint(v), false, false);
  return fmaxf(__uint_as_float(r[0]), __uint_as_float(r[1]));
}
DI bf16x8 pack8(const f32x16& a, int s) {
  u32x4 w = {pk2(a[8 * s], a[8 * s + 1]), pk2(a[8 * s + 2], a[8 * s + 3]), pk2(a[8 * s + 4], a[8 * s + 5]),
             pk2(a[8 * s + 6], a[8 * s + 7])};
  return __builtin_bit_cast(bf16x8, w);
}
DI void glds16(const void* g, void* l) {
  __builtin_amdgcn_global_load_lds((const __attribute__((address_space(1))) unsigned*)g, (LDS_AS unsigned*)l, 16, 0, 0);
}
template <int OFF>
DI void glds16o(const char* ubase, unsigned voff, void* l) {
  __builtin_amdgcn_global_load_lds((const __attribute__((address_space(1))) unsigned*)(ubase + voff), (LDS_AS unsigned*)l, 16, OFF, 0);
}
DI s16x4 tr_read(const char* p) { return __builtin_amdgcn_ds_read_tr16_b64_v4i16((LDS_AS s16x4*)p); }
DI u32x2 neg_split3(float m) {
  float n = -m;
  float a = rbf(n), r1 = n - a, b = rbf(r1), r2 = r1 - b;
  return u32x2{pk2(a, b), pk2(r2, 0.f)};
}
constexpr unsigned ONES2 = 0x3f803f80u;
template <int N> struct IC { static constexpr int value = N; };

constexpr int K1_WIMG = 448 * 64;
constexpr int K1_XOFF = 3 * K1_WIMG;
constexpr int K1_LDS = K1_XOFF + 4 * 4 * 4096;

__global__ __launch_bounds__(256, 2) void k1_qkv(const uint16_t* __restrict__ x, const uint16_t* __restrict__ wqkv,
                                                 char* __restrict__ ws) {
  extern __shared__ __attribute__((aligned(16))) char sm[];
  const int tid = threadIdx.x, lane = tid & 63, w = __builtin_amdgcn_readfirstlane(tid >> 6);
  const int b = blockIdx.x, xcd = b & 7, idx = b >> 3;
  const int tt = (xcd >> 1) * 4 + (idx & 3), h = (xcd & 1) * 8 + (idx >> 2);
  const int r = lane & 31, hh = lane >> 5;
  TS_DECL; TS(0);
  const char* wsrc = (const char*)wqkv + (size_t)(16 * w + (lane >> 2)) * 2688 + ((56 * h) & ~15) + (lane & 3) * 16;
  const char* xsrc0 = (const char*)x + (size_t)(tt * 128 + w * 32 + (lane >> 3)) * 896 + (((lane & 7) ^ (lane >> 4)) * 16);
  const char* xsrc1 = (const char*)x + (size_t)(tt * 128 + w * 32 + 8 + (lane >> 3)) * 896 + (((lane & 7) ^ (4 + (lane >> 4))) * 16);
  char* xl = sm + K1_XOFF + w * 16384;
#define K1_LDW(kc)                                                                                          \
  _Pragma("unroll") for (int s = 0; s < 3; ++s)                                                             \
      glds16(wsrc + (size_t)(kc) * 64 * 2688 + s * 896, sm + s * K1_WIMG + ((kc) * 64 + 16 * w) * 64);
#define K1_LDX(kc)                                                                                          \
  _Pragma("unroll") for (int t = 0; t < 2; ++t) {                                                           \
    glds16(xsrc0 + (size_t)t * 16 * 896 + (kc) * 128, xl + ((kc) & 3) * 4096 + t * 2048);                   \
    glds16(xsrc1 + (size_t)t * 16 * 896 + (kc) * 128, xl + ((kc) & 3) * 4096 + t * 2048 + 1024);            \
  }
  K1_LDW(0) K1_LDX(0) K1_LDW(1) K1_LDX(1) K1_LDW(2) K1_LDX(2) K1_LDW(3) K1_LDX(3) K1_LDW(4) K1_LDW(5) K1_LDW(6)

  TS(1);
  const unsigned lbase = (unsigned)(uintptr_t)(LDS_AS char*)sm;
  const int xsw = (r >> 1) & 7;
  unsigned xa[4], wa[4], wb[4];
#pragma unroll
  for (int s = 0; s < 4; ++s) {
    const int sr = (s + w) & 3;
    xa[s] = lbase + K1_XOFF + w * 16384 + r * 128 + (((2 * sr + hh) ^ xsw) * 16);
    wa[s] = lbase + (16 * sr + 8 * hh + ((lane & 15) >> 2)) * 64 + 8 * (h & 1) + 32 * ((lane >> 4) & 1) + 8 * (lane & 3);
    wb[s] = wa[s] + 2 * K1_WIMG;
  }
  f32x16 aq = {}, ak = {}, av = {};
  float sx = 0.f, sxx = 0.f, csq = 0.f, csk = 0.f, csv = 0.f;
  bf16x8 xf[3];
  s16x4 wl[3][6];
#define K1_RD(n)                                                                                             \
  {                                                                                                          \
    constexpr int kc_ = (n) >> 2, s_ = (n) & 3, b_ = (n) % 3;                                                \
    asm volatile("ds_read_b128 %0, %1 offset:%2" : "=v"(xf[b_]) : "v"(xa[s_]), "i"((kc_ & 3) * 4096));       \
    asm volatile("ds_read_b64_tr_b16 %0, %1 offset:%2" : "=v"(wl[b_][0]) : "v"(wa[s_]), "i"(kc_ * 4096));    \
    asm volatile("ds_read_b64_tr_b16 %0, %1 offset:%2" : "=v"(wl[b_][1]) : "v"(wa[s_]), "i"(kc_ * 4096 + 256)); \
    asm volatile("ds_read_b64_tr_b16 %0, %1 offset:%2" : "=v"(wl[b_][2]) : "v"(wa[s_]), "i"(K1_WIMG + kc_ * 4096)); \
    asm volatile("ds_read_b64_tr_b16 %0, %1 offset:%2" : "=v"(wl[b_][3]) : "v"(wa[s_]), "i"(K1_WIMG + kc_ * 4096 + 256)); \
    asm volatile("ds_read_b64_tr_b16 %0, %1 offset:%2" : "=v"(wl[b_][4]) : "v"(wb[s_]), "i"(kc_ * 4096));    \
    asm volatile("ds_read_b64_tr_b16 %0, %1 offset:%2" : "=v"(wl[b_][5]) : "v"(wb[s_]), "i"(kc_ * 4096 + 256)); \
  }
  WAIT_VM(30);
  BAR();
  asm volatile("" ::: "memory");
  TS(2);
  K1_RD(0) K1_RD(1)
  auto step = [&](auto nn) __attribute__((always_inline)) {
    constexpr int n = decltype(nn)::value, kc = n >> 2, s = n & 3, bb = n % 3;
    if (n + 2 < 28) {
      if (s == 2) {
        if (kc == 0) WAIT_VM(23);
        else if (kc == 1) WAIT_VM(20);
        else if (kc == 2) WAIT_VM(17);
        else if (kc == 3) WAIT_VM(8);
        else if (kc == 4) WAIT_VM(4);
        else WAIT_VM(0);
        if (kc < 4) BAR();
        asm volatile("" ::: "memory");
      }
      K1_RD(n + 2)
      asm volatile("s_waitcnt lgkmcnt(14)" : "+v"(xf[bb]), "+v"(wl[bb][0]), "+v"(wl[bb][1]), "+v"(wl[bb][2]), "+v"(wl[bb][3]), "+v"(wl[bb][4]), "+v"(wl[bb][5]));
    } else if (n == 26) {
      asm volatile("s_waitcnt lgkmcnt(7)" : "+v"(xf[bb]), "+v"(wl[bb][0]), "+v"(wl[bb][1]), "+v"(wl[bb][2]), "+v"(wl[bb][3]), "+v"(wl[bb][4]), "+v"(wl[bb][5]));
    } else {
      asm volatile("s_waitcnt lgkmcnt(0)" : "+v"(xf[bb]), "+v"(wl[bb][0]), "+v"(wl[bb][1]), "+v"(wl[bb][2]), "+v"(wl[bb][3]), "+v"(wl[bb][4]), "+v"(wl[bb][5]));
    }
    SB();
    if (s == 3 && kc < 3) {
      if (kc == 0) { K1_LDX(4) } else if (kc == 1) { K1_LDX(5) } else { K1_LDX(6) }
    }
    const bf16x8 w0 = __builtin_shufflevector(wl[bb][0], wl[bb][1], 0, 1, 2, 3, 4, 5, 6, 7);
    const bf16x8 w1 = __builtin_shufflevector(wl[bb][2], wl[bb][3], 0, 1, 2, 3, 4, 5, 6, 7);
    const bf16x8 w2 = __builtin_shufflevector(wl[bb][4], wl[bb][5], 0, 1, 2, 3, 4, 5, 6, 7);
    aq = MFMA(w0, xf[bb], aq);
    ak = MFMA(w1, xf[bb], ak);
    av = MFMA(xf[bb], w2, av);
    const u32x4 xu = __builtin_bit_cast(u32x4, xf[bb]);
#pragma unroll
    for (int j = 0; j < 4; ++j) { sx = dot2(xu[j], ONES2, sx); sxx = dot2(xu[j], xu[j], sxx); }
    if (s == 0) {
      const u32x4 u0 = __builtin_bit_cast(u32x4, w0), u1 = __builtin_bit_cast(u32x4, w1), u2 = __builtin_bit_cast(u32x4, w2);
#pragma unroll
      for (int j = 0; j < 4; ++j) { csq = dot2(u0[j], ONES2, csq); csk = dot2(u1[j], ONES2, csk); csv = dot2(u2[j], ONES2, csv); }
    }
    SB();
    if (n == 3) TS(3);
    if (n == 15) TS(4);
  };
#define K1_S4(k) step(IC<4 * k>{}); step(IC<4 * k + 1>{}); step(IC<4 * k + 2>{}); step(IC<4 * k + 3>{});
  K1_S4(0) K1_S4(1) K1_S4(2) K1_S4(3) K1_S4(4) K1_S4(5) K1_S4(6)
  TS(5);
  sx = xhalf_add(sx); sxx = xhalf_add(sxx);
  csq = xhalf_add(csq); csk = xhalf_add(csk); csv = xhalf_add(csv);
  const float mean = sx * (1.f / 448.f);
  const float var = fmaxf(sxx * (1.f / 448.f) - mean * mean, 0.f);
  const float rstd = __builtin_amdgcn_rsqf(var + LN_EPS);
  float* tb = (float*)xl;
  WAIT_LGKM0();
  if (hh == 0) { tb[r] = csq; tb[32 + r] = csk; tb[64 + r] = csv; tb[96 + r] = mean; tb[128 + r] = rstd; }
  __syncthreads();
  const float* t0 = (const float*)(sm + K1_XOFF);
  const size_t tile = (size_t)h * 64 + tt * 4 + w;
  float cv = 0.f;
#pragma unroll
  for (int ww = 0; ww < 4; ++ww) cv += t0[ww * 4096 + 64 + r];
  float sii = 0.f;
  u32x4 qo[2], ko[2], vo[2];
#pragma unroll
  for (int g = 0; g < 4; ++g) {
    f32x4 cq = {}, ck = {};
#pragma unroll
    for (int ww = 0; ww < 4; ++ww) {
      cq += *(const f32x4*)(t0 + ww * 4096 + 8 * g + 4 * hh);
      ck += *(const f32x4*)(t0 + ww * 4096 + 32 + 8 * g + 4 * hh);
    }
    const f32x4 mu = *(const f32x4*)(tb + 96 + 8 * g + 4 * hh), rs = *(const f32x4*)(tb + 128 + 8 * g + 4 * hh);
    float q[4], k[4], v[4];
#pragma unroll
    for (int j = 0; j < 4; ++j) {
      q[j] = (aq[4 * g + j] - mean * cq[j]) * (rstd * QSCALE);
      k[j] = (ak[4 * g + j] - mean * ck[j]) * rstd;
      v[j] = (av[4 * g + j] - mu[j] * cv) * rs[j];
      if (r >= 28) v[j] = (r == 28) ? 1.f : 0.f;
    }
    unsigned q0 = pk2(q[0], q[1]), q1 = pk2(q[2], q[3]), k0 = pk2(k[0], k[1]), k1 = pk2(k[2], k[3]);
    if (g == 3 && hh == 1) { q0 = 0; q1 = 0; k0 = ONES2; k1 = 0x00003f80u; }
    sii = dot2(q0, k0, sii); sii = dot2(q1, k1, sii);
    qo[g >> 1][2 * (g & 1)] = q0; qo[g >> 1][2 * (g & 1) + 1] = q1;
    ko[g >> 1][2 * (g & 1)] = k0; ko[g >> 1][2 * (g & 1) + 1] = k1;
    vo[g >> 1][2 * (g & 1)] = pk2(v[0], v[1]); vo[g >> 1][2 * (g & 1) + 1] = pk2(v[2], v[3]);
  }
  sii = xhalf_add(sii);
  if (hh == 1) { u32x2 ms = neg_split3(sii); qo[1][2] = ms[0]; qo[1][3] = ms[1]; }
  char* qd = ws + WS_QF + tile * 2048 + lane * 16;
  char* kd = ws + WS_KVF + tile * 4096 + lane * 16;
  *(u32x4*)(qd) = qo[0]; *(u32x4*)(qd + 1024) = qo[1];
  *(u32x4*)(kd) = ko[0]; *(u32x4*)(kd + 1024) = ko[1];
  *(u32x4*)(kd + 2048) = vo[0]; *(u32x4*)(kd + 3072) = vo[1];
  TS_OUT(0);
}

constexpr int K2_QL = 8 * 16384;
constexpr int K2_LS = K2_QL + 8192;
constexpr int K2_LDS = K2_LS + 4096;

__global__ __launch_bounds__(512, 2) void k2_attn(const uint16_t* __restrict__ wo, const uint16_t* __restrict__ wg,
                                                  const uint16_t* __restrict__ wu, const uint16_t* __restrict__ wd,
                                                  char* __restrict__ ws) {
  extern __shared__ __attribute__((aligned(16))) char sm[];
  const int tid = threadIdx.x, lane = tid & 63, w = __builtin_amdgcn_readfirstlane(tid >> 6);
  const int b = blockIdx.x, xcd = b & 7, idx = b >> 3;
  const int h = 2 * xcd + (idx & 1), qt = idx >> 1;
  const int r = lane & 31, hh = lane >> 5;
  TS_DECL; TS(0);
  const unsigned l16 = lane * 16;
  const unsigned lbase = (unsigned)(uintptr_t)(LDS_AS char*)sm;
  glds16o<0>(ws + WS_QF + ((size_t)h * 64 + qt * 4) * 2048 + w * 1024, l16, sm + K2_QL + w * 1024);
  const char* kvsrc = ws + WS_KVF + ((size_t)h * 64 + w * 8) * 4096;
  bf16x8 kv[8][4];
#pragma unroll
  for (int t = 0; t < 8; ++t)
#pragma unroll
    for (int c = 0; c < 4; ++c)
      asm volatile("global_load_dwordx4 %0, %1, %2 offset:%3" : "=v"(kv[t][c]) : "v"(l16), "s"(kvsrc + t * 4096), "i"(c * 1024) : "memory");
  SB();
  char* wdst[3];
  {
    const int gw = b * 8 + w;
#pragma unroll
    for (int it = 0; it < 3; ++it) {
      int f = gw + it * 2048;
      if (f >= 5096) f = gw;
      const uint16_t* src;
      if (f < 3136) {
        const int cl = f & 3, t = f >> 2, ks = t % 28, c = (t / 28) * 4 + cl;
        src = ((lane & 2) ? wu : wg) + (size_t)(16 * ks + (lane >> 2)) * 1792 + 16 * c + 8 * (lane & 1);
        wdst[it] = ws + WS_WGF + ((size_t)c * 28 + ks) * 1024;
      } else if (f < 3136 + 1568) {
        const int g = f - 3136, nl = g & 1, t = g >> 1, ks = t % 112, nb = (t / 112) * 2 + nl;
        src = wd + (size_t)(16 * ks + (lane >> 2)) * 448 + 32 * nb + 8 * (lane & 3);
        wdst[it] = ws + WS_WDF + ((size_t)nb * 112 + ks) * 1024;
      } else {
        const int g = f - 4704, nl = g & 1, t = g >> 1, ks = t % 28, nb = (t / 28) * 2 + nl;
        src = wo + (size_t)(16 * ks + (lane >> 2)) * 448 + 32 * nb + 8 * (lane & 3);
        wdst[it] = ws + WS_WOF + ((size_t)nb * 28 + ks) * 1024;
      }
      glds16(src, sm + w * 16384 + 12288 + it * 1024);
    }
  }
  TS(1);
  WAIT_VM(35);
  BAR();
  asm volatile("" ::: "memory");
  TS(2);
  bf16x8 q[4][2];
  const unsigned qa = lbase + K2_QL + l16;
#define K2_RDQ(i)                                                                                            \
  asm volatile("ds_read_b128 %0, %1 offset:%2" : "=v"(q[i][0]) : "v"(qa), "i"((i) * 2048));                  \
  asm volatile("ds_read_b128 %0, %1 offset:%2" : "=v"(q[i][1]) : "v"(qa), "i"((i) * 2048 + 1024));
#define K2_WTQ(i) asm volatile("s_waitcnt lgkmcnt(0)" : "+v"(q[i][0]), "+v"(q[i][1]));
  K2_RDQ(0) K2_RDQ(1)
  K2_WTQ(0) K2_WTQ(1)
  float* ls = (float*)(sm + K2_LS);
  auto put = [&](const f32x16& o, int i) __attribute__((always_inline)) {
#pragma unroll
    for (int g = 0; g < 4; ++g)
      *(f32x4*)(sm + w * 16384 + (i * 4 + g) * 1024 + l16) = f32x4{o[4 * g], o[4 * g + 1], o[4 * g + 2], o[4 * g + 3]};
    if (hh == 1) ls[w * 128 + i * 32 + r] = o[12];
  };
  f32x16 o[2], sc[2];
  asm volatile("s_waitcnt vmcnt(33)" : "+v"(kv[0][0]), "+v"(kv[0][1]));
  sc[0] = MFMA(kv[0][0], q[0][0], (f32x16{}));
  sc[0] = MFMA(kv[0][1], q[0][1], sc[0]);
  auto tile = [&](auto nn) __attribute__((always_inline)) {
    constexpr int n = decltype(nn)::value, m = n + 1;
    constexpr int t = n < 16 ? n >> 1 : n & 7, i = n < 16 ? n & 1 : n >> 3;
    constexpr int tn = m < 16 ? m >> 1 : m & 7, in = m < 16 ? m & 1 : (m >> 3) & 3;
    f32x16& s = sc[n & 1];
    f32x16& sn = sc[m & 1];
    f32x16& oo = o[i & 1];
    if (n == 13) { K2_RDQ(2) }
    if (n == 21) { K2_RDQ(3) }
    if (n == 15) { K2_WTQ(2) }
    if (n == 23) { K2_WTQ(3) }
    if (n == 0) asm volatile("s_waitcnt vmcnt(31)" : "+v"(kv[0][2]), "+v"(kv[0][3]));
    if (n < 15 && (n & 1))
      asm volatile("s_waitcnt vmcnt(%4)" : "+v"(kv[tn][0]), "+v"(kv[tn][1]), "+v"(kv[tn][2]), "+v"(kv[tn][3]) : "i"((7 - tn) * 4 + 3));
    SB();
    if (m < 32) sn = MFMA(kv[tn][0], q[in][0], (f32x16{}));
    SB();
#pragma unroll
    for (int e = 0; e < 4; ++e) s[e] = __builtin_amdgcn_exp2f(s[e]);
    SB();
    if (m < 32) sn = MFMA(kv[tn][1], q[in][1], sn);
    SB();
#pragma unroll
    for (int e = 4; e < 8; ++e) s[e] = __builtin_amdgcn_exp2f(s[e]);
    const bf16x8 p0 = pack8(s, 0);
    SB();
    if (t == 0) oo = MFMA(kv[t][2], p0, (f32x16{})); else oo = MFMA(kv[t][2], p0, oo);
    SB();
#pragma unroll
    for (int e = 8; e < 16; ++e) s[e] = __builtin_amdgcn_exp2f(s[e]);
    const bf16x8 p1 = pack8(s, 1);
    SB();
    oo = MFMA(kv[t][3], p1, oo);
    SB();
    if (n == 15) put(o[0], 0);
    if (n == 16) put(o[1], 1);
    if (n == 24) put(o[0], 2);
    if (n == 31) put(o[1], 3);
    if (n == 18) {
      WAIT_VM(0);
      const unsigned wa = lbase + w * 16384 + 12288 + (4 * hh + ((lane & 15) >> 2)) * 64 + 32 * ((lane >> 4) & 1) + 8 * (lane & 3);
      s16x4 wl[3][2];
#pragma unroll
      for (int it = 0; it < 3; ++it) {
        asm volatile("ds_read_b64_tr_b16 %0, %1 offset:%2" : "=v"(wl[it][0]) : "v"(wa), "i"(it * 1024));
        asm volatile("ds_read_b64_tr_b16 %0, %1 offset:%2" : "=v"(wl[it][1]) : "v"(wa), "i"(it * 1024 + 512));
      }
      asm volatile("s_waitcnt lgkmcnt(0)" : "+v"(wl[0][0]), "+v"(wl[0][1]), "+v"(wl[1][0]), "+v"(wl[1][1]), "+v"(wl[2][0]), "+v"(wl[2][1]) :: "memory");
#pragma unroll
      for (int it = 0; it < 3; ++it)
        if (it < 2 || b * 8 + w + 4096 < 5096)
          *(bf16x8*)(wdst[it] + l16) = __builtin_shufflevector(wl[it][0], wl[it][1], 0, 1, 2, 3, 4, 5, 6, 7);
    }
    SB();
    if (n == 1) TS(3);
    if (n == 15) TS(4);
  };
#define K2_T4(k) tile(IC<4 * k>{}); tile(IC<4 * k + 1>{}); tile(IC<4 * k + 2>{}); tile(IC<4 * k + 3>{});
  K2_T4(0) K2_T4(1) K2_T4(2) K2_T4(3) K2_T4(4) K2_T4(5) K2_T4(6) K2_T4(7)
  TS(5);
  const int oi = w >> 1, og = 2 * (w & 1);
  f32x4 res[2];
  bool bad;
  auto gather = [&]() __attribute__((always_inline)) {
    __syncthreads();
    float l = 0.f;
    f32x2 la = {};
    res[0] = f32x4{}; res[1] = f32x4{};
#pragma unroll
    for (int ww = 0; ww < 8; ++ww) {
      res[0] += *(const f32x4*)(sm + ww * 16384 + (oi * 4 + og) * 1024 + l16);
      res[1] += *(const f32x4*)(sm + ww * 16384 + (oi * 4 + og + 1) * 1024 + l16);
      l += ls[ww * 128 + oi * 32 + r];
      la += *(const f32x2*)(ls + ww * 128 + 2 * lane);
    }
    const float rl = 1.f / l;
    res[0] *= rl; res[1] *= rl;
    bad = __any(!(fmaxf(la[0], la[1]) < 1.0e9f) || !(la[0] == la[0]) || !(la[1] == la[1]));
  };
  gather();
  TS(6);
  if (bad) {
    __syncthreads();
    u32x4 qq[4][2];
#pragma unroll
    for (int i = 0; i < 4; ++i) {
      qq[i][0] = *(const u32x4*)(sm + K2_QL + i * 2048 + l16);
      qq[i][1] = *(const u32x4*)(sm + K2_QL + i * 2048 + 1024 + l16);
      if (hh == 1) { qq[i][1][2] = 0; qq[i][1][3] = 0; }
    }
    float m[4] = {-INFINITY, -INFINITY, -INFINITY, -INFINITY};
#pragma unroll 1
    for (int t = 0; t < 8; ++t) {
      const bf16x8 k0 = *(const bf16x8*)(kvsrc + t * 4096 + l16), k1 = *(const bf16x8*)(kvsrc + t * 4096 + 1024 + l16);
#pragma unroll
      for (int i = 0; i < 4; ++i) {
        f32x16 s = {};
        s = MFMA(k0, __builtin_bit_cast(bf16x8, qq[i][0]), s);
        s = MFMA(k1, __builtin_bit_cast(bf16x8, qq[i][1]), s);
#pragma unroll
        for (int e = 0; e < 16; ++e) m[i] = fmaxf(m[i], s[e]);
      }
    }
#pragma unroll
    for (int i = 0; i < 4; ++i) { m[i] = xhalf_max(m[i]); if (hh == 0) ls[w * 128 + i * 32 + r] = m[i]; }
    __syncthreads();
#pragma unroll
    for (int i = 0; i < 4; ++i) {
      float mm = -INFINITY;
#pragma unroll
      for (int ww = 0; ww < 8; ++ww) mm = fmaxf(mm, ls[ww * 128 + i * 32 + r]);
      const u32x2 ms = neg_split3(mm);
      if (hh == 1) { qq[i][1][2] = ms[0]; qq[i][1][3] = ms[1]; }
    }
    __syncthreads();
    f32x16 of[4] = {};
#pragma unroll 1
    for (int t = 0; t < 8; ++t) {
      const bf16x8 k0 = *(const bf16x8*)(kvsrc + t * 4096 + l16), k1 = *(const bf16x8*)(kvsrc + t * 4096 + 1024 + l16);
      const bf16x8 v0 = *(const bf16x8*)(kvsrc + t * 4096 + 2048 + l16), v1 = *(const bf16x8*)(kvsrc + t * 4096 + 3072 + l16);
#pragma unroll
      for (int i = 0; i < 4; ++i) {
        f32x16 s = {};
        s = MFMA(k0, __builtin_bit_cast(bf16x8, qq[i][0]), s);
        s = MFMA(k1, __builtin_bit_cast(bf16x8, qq[i][1]), s);
#pragma unroll
        for (int e = 0; e < 16; ++e) s[e] = __builtin_amdgcn_exp2f(s[e]);
        of[i] = MFMA(v0, pack8(s, 0), of[i]);
        of[i] = MFMA(v1, pack8(s, 1), of[i]);
      }
    }
#pragma unroll
    for (int i = 0; i < 4; ++i) put(of[i], i);
    gather();
  }
#pragma unroll
  for (int c = 0; c < 2; ++c) {
    const int gl = 2 * (og + c) + hh;
    const int gi = 7 * h + gl;
    if (gl < 7) {
      char* od = ws + WS_OF + ((size_t)(qt * 4 + oi) * 28 + (gi >> 2)) * 1024 + ((gi & 1) * 32 + r) * 16 + ((gi >> 1) & 1) * 8;
      *(u32x2*)od = u32x2{pk2(res[c][0], res[c][1]), pk2(res[c][2], res[c][3])};
    }
  }
  TS_OUT(1);
}

template <int T>
DI void red_put(const f32x16& a, char* sm, int w, int lane) {
#pragma unroll
  for (int g = 0; g < 4; ++g)
    *(f32x4*)(sm + ((w * 4 + T) * 4 + g) * 1024 + lane * 16) = f32x4{a[4 * g], a[4 * g + 1], a[4 * g + 2], a[4 * g + 3]};
}
DI f32x16 reduce4(f32x16 (&acc)[2][2], char* sm, int w, int lane) {
  f32x16 o;
  if (w != 0) red_put<0>(acc[0][0], sm, w, lane); else o = acc[0][0];
  if (w != 1) red_put<1>(acc[0][1], sm, w, lane); else o = acc[0][1];
  if (w != 2) red_put<2>(acc[1][0], sm, w, lane); else o = acc[1][0];
  if (w != 3) red_put<3>(acc[1][1], sm, w, lane); else o = acc[1][1];
  __syncthreads();
  f32x4 p[3][4];
#pragma unroll
  for (int i = 0; i < 3; ++i) {
    const int ww = (w + 1 + i) & 3;
#pragma unroll
    for (int g = 0; g < 4; ++g) p[i][g] = *(const f32x4*)(sm + ((ww * 4 + w) * 4 + g) * 1024 + lane * 16);
  }
#pragma unroll
  for (int g = 0; g < 4; ++g)
#pragma unroll
    for (int j = 0; j < 4; ++j) o[4 * g + j] = (o[4 * g + j] + p[0][g][j]) + (p[1][g][j] + p[2][g][j]);
  return o;
}

__global__ __launch_bounds__(256, 1) void k3_oproj(const uint16_t* __restrict__ x, char* __restrict__ ws) {
  extern __shared__ __attribute__((aligned(16))) char sm[];
  const int tid = threadIdx.x, lane = tid & 63, w = __builtin_amdgcn_readfirstlane(tid >> 6);
  const int b = blockIdx.x, r = lane & 31, hh = lane >> 5;
  TS_DECL; TS(0);
  if (b >= 224) {
    const int c = (b - 224) * 4 + w;
    const char* src = ws + WS_WGF + (size_t)c * 28 * 1024 + lane * 16;
    u32x4 v[28];
#pragma unroll
    for (int i = 0; i < 28; ++i) v[i] = *(const u32x4*)(src + i * 1024);
    SB();
    float s0 = 0.f, s1 = 0.f;
#pragma unroll
    for (int i = 0; i < 28; ++i) { s0 = dot2(v[i][0], ONES2, s0); s1 = dot2(v[i][1], ONES2, s1); s0 = dot2(v[i][2], ONES2, s0); s1 = dot2(v[i][3], ONES2, s1); }
    const float s = xhalf_add(s0 + s1);
    if (hh == 0) ((float*)(ws + WS_CS))[c * 32 + r] = s;
    TS_OUT(2);
    return;
  }
  const int xcd = b & 7, idx = b >> 3, t64 = 4 * xcd + (idx & 3), nb = idx >> 2;
  const char* osrc = ws + WS_OF + ((size_t)(2 * t64) * 28 + 7 * w) * 1024 + lane * 16;
  const char* wsrc = ws + WS_WOF + ((size_t)(2 * nb) * 28 + 7 * w) * 1024 + lane * 16;
  bf16x8 of[7][2], wf[7][2];
#pragma unroll
  for (int s = 0; s < 7; ++s) {
    of[s][0] = *(const bf16x8*)(osrc + s * 1024); wf[s][0] = *(const bf16x8*)(wsrc + s * 1024);
    of[s][1] = *(const bf16x8*)(osrc + (28 + s) * 1024); wf[s][1] = *(const bf16x8*)(wsrc + (28 + s) * 1024);
  }
  const int tt32 = 2 * t64 + (w & 1), n32 = 2 * nb + (w >> 1);
  const uint16_t* xs = x + (size_t)(tt32 * 32 + r) * 448 + n32 * 32 + 4 * hh;
  u32x2 xr[4];
#pragma unroll
  for (int g = 0; g < 4; ++g) xr[g] = *(const u32x2*)(xs + 8 * g);
  SB();
  TS(1);
  f32x16 acc[2][2] = {};
#pragma unroll
  for (int s = 0; s < 7; ++s) {
#pragma unroll
    for (int c = 0; c < 2; ++c)
#pragma unroll
      for (int a = 0; a < 2; ++a) acc[c][a] = MFMA(wf[s][c], of[s][a], acc[c][a]);
    SB();
  }
  TS(2);
  f32x16 o = reduce4(acc, sm, w, lane);
  TS(3);
  float s1 = 0.f, s2 = 0.f;
  u32x4 ho[2];
#pragma unroll
  for (int g = 0; g < 4; ++g) {
    const float h0 = bf_lo(xr[g][0]) + rbf(o[4 * g]), h1 = bf_hi(xr[g][0]) + rbf(o[4 * g + 1]);
    const float h2 = bf_lo(xr[g][1]) + rbf(o[4 * g + 2]), h3 = bf_hi(xr[g][1]) + rbf(o[4 * g + 3]);
    const unsigned p0 = pk2(h0, h1), p1 = pk2(h2, h3);
    s1 = dot2(p0, ONES2, s1); s1 = dot2(p1, ONES2, s1);
    s2 = dot2(p0, p0, s2); s2 = dot2(p1, p1, s2);
    ho[g >> 1][2 * (g & 1)] = p0; ho[g >> 1][2 * (g & 1) + 1] = p1;
  }
  char* hd = ws + WS_HF + ((size_t)tt32 * 28 + 2 * n32) * 1024 + lane * 16;
  *(u32x4*)hd = ho[0]; *(u32x4*)(hd + 1024) = ho[1];
  s1 = xhalf_add(s1); s2 = xhalf_add(s2);
  if (hh == 0) *(f32x2*)(ws + WS_ST + ((size_t)n32 * 2048 + tt32 * 32 + r) * 8) = f32x2{s1, s2};
  TS_OUT(2);
}

constexpr int K4_CH = 28 * 1024;
constexpr int K4_ST = 5 * K4_CH;
constexpr int K4_CS = K4_ST + 14 * 1024;
constexpr int K4_LDS = K4_CS + 1024;

__global__ __launch_bounds__(256, 2) void k4_ffn_up(char* __restrict__ ws) {
  extern __shared__ __attribute__((aligned(16))) char sm[];
  const int tid = threadIdx.x, lane = tid & 63, w = __builtin_amdgcn_readfirstlane(tid >> 6);
  const int b = blockIdx.x, xcd = b & 7, idx = b >> 3;
  const int tt = (xcd & 1) * 8 + (idx & 7), cg = (xcd >> 1) * 4 + (idx >> 3);
  const int r = lane & 31, hh = lane >> 5;
  TS_DECL; TS(0);
  const char* wsrc = ws + WS_WGF + (size_t)cg * 7 * K4_CH + w * 7168;
  const char* hsrc = ws + WS_HF + ((size_t)(tt * 4 + w) * 28) * 1024;
  const unsigned l16 = lane * 16;
#define K4_LDW(c)                                                                                            \
  {                                                                                                          \
    const char* g_ = wsrc + (c) * K4_CH; char* l_ = sm + ((c) % 5) * K4_CH + w * 7168;                       \
    glds16o<0>(g_, l16, l_); glds16o<1024>(g_, l16, l_); glds16o<2048>(g_, l16, l_); glds16o<3072>(g_, l16, l_); \
    glds16o<0>(g_ + 4096, l16, l_ + 4096); glds16o<1024>(g_ + 4096, l16, l_ + 4096); glds16o<2048>(g_ + 4096, l16, l_ + 4096); \
  }
  K4_LDW(0)
  bf16x8 hf[28];
#pragma unroll
  for (int i = 0; i < 28; ++i)
    asm volatile("global_load_dwordx4 %0, %1, %2 offset:%3" : "=v"(hf[i]) : "v"(l16), "s"(hsrc + (i >> 2) * 4096), "i"((i & 3) * 1024) : "memory");
  {
    const char* st = ws + WS_ST + (size_t)tt * 1024 + lane * 16;
#pragma unroll
    for (int i = 0; i < 4; ++i) {
      const int n = w * 4 + i;
      const char* src = n < 14 ? st + (size_t)n * 16384 : ws + WS_CS + cg * 896 + lane * 16;
      glds16(src, sm + K4_ST + (n < 14 ? n : 14) * 1024);
    }
  }
  K4_LDW(1) K4_LDW(2) K4_LDW(3)
  TS(1);
  asm volatile("s_waitcnt vmcnt(21)"
               : "+v"(hf[0]), "+v"(hf[1]), "+v"(hf[2]), "+v"(hf[3]), "+v"(hf[4]), "+v"(hf[5]), "+v"(hf[6]), "+v"(hf[7]),
                 "+v"(hf[8]), "+v"(hf[9]), "+v"(hf[10]), "+v"(hf[11]), "+v"(hf[12]), "+v"(hf[13])
               :: "memory");
  asm volatile(""
               : "+v"(hf[14]), "+v"(hf[15]), "+v"(hf[16]), "+v"(hf[17]), "+v"(hf[18]), "+v"(hf[19]), "+v"(hf[20]), "+v"(hf[21]),
                 "+v"(hf[22]), "+v"(hf[23]), "+v"(hf[24]), "+v"(hf[25]), "+v"(hf[26]), "+v"(hf[27])
               :: "memory");
  BAR();
  asm volatile("" ::: "memory");
  TS(2);
  constexpr int PD = 6;
  bf16x8 wf[PD];
  const unsigned lb = (unsigned)(uintptr_t)(LDS_AS char*)sm + l16;
  const unsigned lb0 = lb, lb1 = lb + 2 * K4_CH, lb2 = lb + 4 * K4_CH;
  const unsigned lcs = (unsigned)(uintptr_t)(LDS_AS char*)sm + K4_CS + 16 * hh;
  const unsigned lst = (unsigned)(uintptr_t)(LDS_AS char*)sm + K4_ST + hh * 7168 + (w * 32 + r) * 8;
#define K4_RD(dst, c, ks)                                                                                    \
  {                                                                                                          \
    constexpr int sl_ = (c) % 5;                                                                             \
    asm volatile("ds_read_b128 %0, %1 offset:%2" : "=v"(dst) : "v"(sl_ < 2 ? lb0 : sl_ < 4 ? lb1 : lb2), "i"((sl_ & 1) * K4_CH + (ks) * 1024)); \
  }
  f32x2 st[7];
#pragma unroll
  for (int i = 0; i < PD; ++i) K4_RD(wf[i], 0, i)
#pragma unroll
  for (int i = 0; i < 7; ++i) asm volatile("ds_read_b64 %0, %1 offset:%2" : "=v"(st[i]) : "v"(lst), "i"(i * 1024));
  SB();
  K4_LDW(4)
  char* adst = ws + WS_AF + ((size_t)(tt * 4 + w) * 112 + cg * 7) * 1024 + lane * 16;
  constexpr float GC1 = -2.f * 0.7978845608028654f * 1.4426950408889634f, GC3 = GC1 * 0.044715f;
  float rstd = 0.f, nmr = 0.f;
  f32x16 acc[2];
  f32x4 cs[2][4];
  float gt[8], up[8], ez[8];
  unsigned ap[4];
  asm volatile("s_waitcnt lgkmcnt(11)" : "+v"(wf[0]), "+v"(wf[1]));
  SB();
  auto chunk = [&](auto cc) __attribute__((always_inline)) {
    constexpr int c = decltype(cc)::value;
#pragma unroll
    for (int ks = 0; ks < 28; ++ks) {
      if (c < 7) {
        constexpr int g0 = c * 28;
        if (ks == 0) acc[c & 1] = MFMA(wf[(g0 + ks) % PD], hf[ks], (f32x16{}));
        else acc[c & 1] = MFMA(wf[(g0 + ks) % PD], hf[ks], acc[c & 1]);
        SB();
        if (ks + PD == 28 && c < 6) {
          if (c == 0) WAIT_VM(21);
          else if (c == 1) WAIT_VM(21);
          else if (c == 2) WAIT_VM(22);
          else if (c == 3) WAIT_VM(16);
          else if (c == 4) WAIT_VM(10);
          else WAIT_VM(4);
          if (c < 2) WAIT_LGKM0();
          BAR();
          asm volatile("" ::: "memory");
        }
        if (ks + PD < 28) K4_RD(wf[(g0 + ks) % PD], c, ks + PD)
        else if (c < 6) K4_RD(wf[(g0 + ks) % PD], c + 1, ks + PD - 28)
        if (ks + PD == 28) { if (c == 0) { K4_LDW(5) } else if (c == 1) { K4_LDW(6) } }
        if (ks == 9) {
#pragma unroll
          for (int g = 0; g < 4; ++g) asm volatile("ds_read_b128 %0, %1 offset:%2" : "=v"(cs[c & 1][g]) : "v"(lcs), "i"(c * 128 + 32 * g));
        }
        if ((ks & 1) && !(c == 6 && ks == 27)) {
          const int kn = ks + 1;
#define K4_WT(n) asm volatile("s_waitcnt lgkmcnt(" #n ")" : "+v"(wf[(g0 + ks + 1) % PD]), "+v"(wf[(g0 + ks + 2) % PD]))
          if (c == 0 && kn < 6) K4_WT(11);
          else if (kn >= 10 && kn < 16) K4_WT(8);
          else if (c == 6 && kn == 24) K4_WT(2);
          else if (c == 6 && kn == 26) K4_WT(0);
          else K4_WT(4);
          SB();
        }
        if (ks == 16) asm volatile("" : "+v"(cs[c & 1][0]), "+v"(cs[c & 1][1]), "+v"(cs[c & 1][2]), "+v"(cs[c & 1][3]));
      }
      if (c == 0) {
        if (ks == 6) {
          asm volatile("" : "+v"(st[0]), "+v"(st[1]), "+v"(st[2]), "+v"(st[3]), "+v"(st[4]), "+v"(st[5]), "+v"(st[6]));
          float s1 = ((st[0][0] + st[1][0]) + (st[2][0] + st[3][0])) + ((st[4][0] + st[5][0]) + st[6][0]);
          float s2 = ((st[0][1] + st[1][1]) + (st[2][1] + st[3][1])) + ((st[4][1] + st[5][1]) + st[6][1]);
          s1 = xhalf_add(s1); s2 = xhalf_add(s2);
          const float mean = s1 * (1.f / 448.f);
          rstd = __builtin_amdgcn_rsqf(fmaxf(s2 * (1.f / 448.f) - mean * mean, 0.f) + LN_EPS);
          nmr = -mean * rstd;
        }
      } else {
        const f32x16& a = acc[(c - 1) & 1];
        const f32x4(&cv)[4] = cs[(c - 1) & 1];
        if (ks >= 2 && ks < 26) {
          const int e = (ks - 2) / 3, ph = (ks - 2) % 3;
          if (ph == 0) {
            const float tg = nmr * cv[e >> 2][e & 3], tu = nmr * cv[2 + (e >> 2)][e & 3];
            gt[e] = fmaf(a[e], rstd, tg);
            up[e] = fmaf(a[8 + e], rstd, tu);
            if (c < 7) { asm volatile("" : "+v"(gt[e])); asm volatile("" : "+v"(up[e])); }
          } else if (ph == 1) {
            ez[e] = __builtin_amdgcn_exp2f(gt[e] * fmaf(gt[e] * gt[e], GC3, GC1));
            up[e] = gt[e] * up[e];
            if (c < 7) { asm volatile("" : "+v"(ez[e])); asm volatile("" : "+v"(up[e])); }
          } else {
            up[e] = up[e] * __builtin_amdgcn_rcpf(1.f + ez[e]);
            if (c < 7) asm volatile("" : "+v"(up[e]));
            if (e & 1) { ap[e >> 1] = pk2(up[e - 1], up[e]); if (c < 7) asm volatile("" : "+v"(ap[e >> 1])); }
          }
        }
        if (ks == 26) *(u32x4*)(adst + (c - 1) * 1024) = u32x4{ap[0], ap[1], ap[2], ap[3]};
      }
      if (c < 7) SB();
    }
    if (c == 0) TS(3);
    if (c == 3) { TS(4); CY0(); }
    if (c == 6) { TS(5); CY1(); }
  };
  chunk(IC<0>{}); chunk(IC<1>{}); chunk(IC<2>{}); chunk(IC<3>{}); chunk(IC<4>{}); chunk(IC<5>{}); chunk(IC<6>{}); chunk(IC<7>{});
  TS_OUT(3);
}

__global__ __launch_bounds__(256, 1) void k5_ffn_down(char* __restrict__ ws, uint16_t* __restrict__ out) {
  extern __shared__ __attribute__((aligned(16))) char sm[];
  const int tid = threadIdx.x, lane = tid & 63, w = __builtin_amdgcn_readfirstlane(tid >> 6);
  const int b = blockIdx.x, r = lane & 31, hh = lane >> 5;
  TS_DECL; TS(0);
  const int xcd = b & 7, idx = b >> 3, t64 = 4 * xcd + (idx & 3), nb = idx >> 2;
  const char* asrc = ws + WS_AF + ((size_t)(2 * t64) * 112 + 28 * w) * 1024 + lane * 16;
  const char* wsrc = ws + WS_WDF + ((size_t)(2 * nb) * 112 + 28 * w) * 1024 + lane * 16;
  constexpr int P = 14;
  bf16x8 af[P][2], wf[P][2];
#pragma unroll
  for (int s = 0; s < P; ++s) {
    af[s][0] = *(const bf16x8*)(asrc + s * 1024); wf[s][0] = *(const bf16x8*)(wsrc + s * 1024);
    af[s][1] = *(const bf16x8*)(asrc + (112 + s) * 1024); wf[s][1] = *(const bf16x8*)(wsrc + (112 + s) * 1024);
  }
  SB();
  TS(1);
  const int tt32 = 2 * t64 + (w & 1), n32 = 2 * nb + (w >> 1);
  const char* hs = ws + WS_HF + ((size_t)tt32 * 28 + 2 * n32) * 1024 + lane * 16;
  u32x4 h0, h1;
  f32x16 acc[2][2] = {};
#pragma unroll
  for (int s = 0; s < 28; ++s) {
#pragma unroll
    for (int c = 0; c < 2; ++c)
#pragma unroll
      for (int a = 0; a < 2; ++a) acc[c][a] = MFMA(wf[s % P][c], af[s % P][a], acc[c][a]);
    SB();
    if (s + P < 28) {
      af[s % P][0] = *(const bf16x8*)(asrc + (s + P) * 1024); wf[s % P][0] = *(const bf16x8*)(wsrc + (s + P) * 1024);
      af[s % P][1] = *(const bf16x8*)(asrc + (112 + s + P) * 1024); wf[s % P][1] = *(const bf16x8*)(wsrc + (112 + s + P) * 1024);
    }
    if (s + P == 28) { h0 = *(const u32x4*)hs; h1 = *(const u32x4*)(hs + 1024); }
    SB();
    if (s == 0) TS(2);
    if (s == 13) TS(3);
  }
  TS(4);
  f32x16 o = reduce4(acc, sm, w, lane);
  TS(5);
  unsigned pq[4][2];
#pragma unroll
  for (int g = 0; g < 4; ++g) {
    const unsigned ha = g < 2 ? h0[2 * (g & 1)] : h1[2 * (g & 1)], hb = g < 2 ? h0[2 * (g & 1) + 1] : h1[2 * (g & 1) + 1];
    pq[g][0] = pk2(bf_lo(ha) + rbf(o[4 * g]), bf_hi(ha) + rbf(o[4 * g + 1]));
    pq[g][1] = pk2(bf_lo(hb) + rbf(o[4 * g + 2]), bf_hi(hb) + rbf(o[4 * g + 3]));
  }
  uint16_t* od = out + (size_t)(tt32 * 32 + r) * 448 + n32 * 32 + 8 * hh;
#pragma unroll
  for (int g = 0; g < 4; g += 2) {
    auto r0 = __builtin_amdgcn_permlane32_swap(pq[g][0], pq[g + 1][0], false, false);
    auto r1 = __builtin_amdgcn_permlane32_swap(pq[g][1], pq[g + 1][1], false, false);
    *(u32x4*)(od + 8 * g) = u32x4{r0[0], r1[0], r0[1], r1[1]};
  }
  TS_OUT(4);
}

extern "C" void kernel_launch(void* const* d_in, const int* in_sizes, int n_in, void* d_out, int out_size, void* d_ws,
                              size_t ws_size, hipStream_t stream) {
  const uint16_t* x = (const uint16_t*)d_in[0];
  const uint16_t* wqkv = (const uint16_t*)d_in[1];
  const uint16_t* wo = (const uint16_t*)d_in[2];
  const uint16_t* wg = (const uint16_t*)d_in[4];
  const uint16_t* wu = (const uint16_t*)d_in[5];
  const uint16_t* wd = (const uint16_t*)d_in[6];
  char* ws = (char*)d_ws;
  (void)hipFuncSetAttribute((const void*)k1_qkv, hipFuncAttributeMaxDynamicSharedMemorySize, K1_LDS);
  (void)hipFuncSetAttribute((const void*)k2_attn, hipFuncAttributeMaxDynamicSharedMemorySize, K2_LDS);
  (void)hipFuncSetAttribute((const void*)k4_ffn_up, hipFuncAttributeMaxDynamicSharedMemorySize, K4_LDS);
  k1_qkv<<<256, 256, K1_LDS, stream>>>(x, wqkv, ws);
  k2_attn<<<256, 512, K2_LDS, stream>>>(wo, wg, wu, wd, ws);
  k3_oproj<<<252, 256, 65536, stream>>>(x, ws);
  k4_ffn_up<<<256, 256, K4_LDS, stream>>>(ws);
  k5_ffn_down<<<224, 256, 65536, stream>>>(ws, (uint16_t*)d_out);
}
```

```cpp
#include <hip/hip_runtime.h>
#include <hip/hip_bf16.h>
#include <stdint.h>

typedef short bf16x8 __attribute__((ext_vector_type(8)));
typedef short s16x4 __attribute__((ext_vector_type(4)));
typedef float f32x16 __attribute__((ext_vector_type(16)));
typedef float f32x4 __attribute__((ext_vector_type(4)));
typedef float f32x2 __attribute__((ext_vector_type(2)));
typedef unsigned u32x4 __attribute__((ext_vector_type(4)));
typedef unsigned u32x2 __attribute__((ext_vector_type(2)));
typedef __bf16 b16x2 __attribute__((ext_vector_type(2)));
#define DI __device__ __forceinline__
#define MFMA(a, b, c) __builtin_amdgcn_mfma_f32_32x32x16_bf16((a), (b), (c), 0, 0, 0)
#define LDS_AS __attribute__((address_space(3)))
#define WAIT_VM(n) asm volatile("s_waitcnt vmcnt(" #n ")" ::: "memory")
#define WAIT_LGKM0() asm volatile("s_waitcnt lgkmcnt(0)" ::: "memory")
#define BAR() __builtin_amdgcn_s_barrier()
#define SB() __builtin_amdgcn_sched_barrier(0)

constexpr int S_ = 2048, D_ = 448, H_ = 16, F_ = 1792;
constexpr float LN_EPS = 1e-5f;
constexpr float QSCALE = 0.18898223650461362f * 1.4426950408889634f;

constexpr size_t WS_QF = 0;
constexpr size_t WS_KVF = WS_QF + (size_t)16 * 64 * 2048;
constexpr size_t WS_OF = WS_KVF + (size_t)16 * 64 * 4096;
constexpr size_t WS_HF = WS_OF + (size_t)64 * 28 * 1024;
constexpr size_t WS_AF = WS_HF + (size_t)64 * 28 * 1024;
constexpr size_t WS_WOF = WS_AF + (size_t)64 * 112 * 1024;
constexpr size_t WS_WGF = WS_WOF + (size_t)14 * 28 * 1024;
constexpr size_t WS_WDF = WS_WGF + (size_t)112 * 28 * 1024;
constexpr size_t WS_ST = WS_WDF + (size_t)14 * 112 * 1024;
constexpr size_t WS_CS = WS_ST + (size_t)14 * 2048 * 8;
constexpr size_t WS_END = WS_CS + 112 * 32 * 4 + 1024;
#define TS_DECL
#define TS(i)
#define TS_OUT(k)
#define CY0()
#define CY1()

DI unsigned pk2(float a, float b) {
  b16x2 v = {(__bf16)a, (__bf16)b};
  return __builtin_bit_cast(unsigned, v);
}
DI float bf_lo(unsigned u) { return __uint_as_float(u << 16); }
DI float bf_hi(unsigned u) { return __uint_as_float(u & 0xffff0000u); }
DI float rbf(float a) { return (float)(__bf16)a; }
DI float dot2(unsigned a, unsigned b, float c) {
  return __builtin_amdgcn_fdot2_f32_bf16(__builtin_bit_cast(b16x2, a), __builtin_bit_cast(b16x2, b), c, false);
}
DI float xhalf_add(float v) {
  auto r = __builtin_amdgcn_permlane32_swap(__float_as_uint(v), __float_as_uint(v), false, false);
  return __uint_as_float(r[0]) + __uint_as_float(r[1]);
}
DI float xhalf_max(float v) {
  auto r = __builtin_amdgcn_permlane32_swap(__float_as_uint(v), __float_as_uint(v), false, false);
  return fmaxf(__uint_as_float(r[0]), __uint_as_float(r[1]));
}
DI bf16x8 pack8(const f32x16& a, int s) {
  u32x4 w = {pk2(a[8 * s], a[8 * s + 1]), pk2(a[8 * s + 2], a[8 * s + 3]), pk2(a[8 * s + 4], a[8 * s + 5]),
             pk2(a[8 * s + 6], a[8 * s + 7])};
  return __builtin_bit_cast(bf16x8, w);
}
DI void glds16(const void* g, void* l) {
  __builtin_amdgcn_global_load_lds((const __attribute__((address_space(1))) unsigned*)g, (LDS_AS unsigned*)l, 16, 0, 0);
}
template <int OFF>
DI void glds16o(const char* ubase, unsigned voff, void* l) {
  __builtin_amdgcn_global_load_lds((const __attribute__((address_space(1))) unsigned*)(ubase + voff), (LDS_AS unsigned*)l, 16, OFF, 0);
}
DI s16x4 tr_read(const char* p) { return __builtin_amdgcn_ds_read_tr16_b64_v4i16((LDS_AS s16x4*)p); }
DI u32x2 neg_split3(float m) {
  float n = -m;
  float a = rbf(n), r1 = n - a, b = rbf(r1), r2 = r1 - b;
  return u32x2{pk2(a, b), pk2(r2, 0.f)};
}
constexpr unsigned ONES2 = 0x3f803f80u;
template <int N> struct IC { static constexpr int value = N; };

constexpr int K1_WIMG = 448 * 64;
constexpr int K1_XOFF = 3 * K1_WIMG;
constexpr int K1_TB = K1_XOFF + 4 * 4 * 4096;
constexpr int K1_DUMMY = K1_TB + 3584;
constexpr int K1_LDS = K1_DUMMY + 256;

DI void split3(float v, float& a, float& b, float& c) { a = rbf(v); const float r1 = v - a; b = rbf(r1); c = r1 - b; }
DI bf16x8 corr_c(float v, int hh) {
  float a, b, c; split3(v, a, b, c);
  u32x4 o = {pk2(a, a), pk2(a, b), pk2(b, b), pk2(c, c)};
  if (hh) o = u32x4{pk2(c, 0.f), 0u, 0u, 0u};
  return __builtin_bit_cast(bf16x8, o);
}
DI bf16x8 corr_m(float v, int hh) {
  float a, b, c; split3(v, a, b, c);
  u32x4 o = {pk2(a, b), pk2(c, a), pk2(b, c), pk2(a, b)};
  if (hh) o = u32x4{pk2(c, 0.f), 0u, 0u, 0u};
  return __builtin_bit_cast(bf16x8, o);
}
DI void st16(void* p, u32x4 v) { asm volatile("global_store_dwordx4 %0, %1, off sc1\n\ts_nop 1" :: "v"(p), "v"(v) : "memory"); }
DI void touch(const void* p, void* dummy) {
  __builtin_amdgcn_global_load_lds((const __attribute__((address_space(1))) unsigned*)p, (LDS_AS unsigned*)dummy, 4, 0, 0);
}
DI void st8(void* p, u32x2 v) { asm volatile("global_store_dwordx2 %0, %1, off sc1" :: "v"(p), "v"(v) : "memory"); }

__global__ __launch_bounds__(512, 2) void k1_qkv(const uint16_t* __restrict__ x, const uint16_t* __restrict__ wqkv,
                                                 char* __restrict__ ws) {
  extern __shared__ __attribute__((aligned(16))) char sm[];
  const int tid = threadIdx.x, lane = tid & 63, w = __builtin_amdgcn_readfirstlane(tid >> 6), wq = w & 3;
  const int b = blockIdx.x, xcd = b & 7, idx = b >> 3;
  const int tt = (xcd >> 1) * 4 + (idx & 3), h = (xcd & 1) * 8 + (idx >> 2);
  const int r = lane & 31, hh = lane >> 5;
  TS_DECL; TS(0);
  const char* wsrc = (const char*)wqkv + (size_t)(16 * wq + (lane >> 2)) * 2688 + ((56 * h) & ~15) + (lane & 3) * 16;
  const char* xsrc0 = (const char*)x + (size_t)(tt * 128 + wq * 32 + (lane >> 3)) * 896 + (((lane & 7) ^ (lane >> 4)) * 16);
  const char* xsrc1 = (const char*)x + (size_t)(tt * 128 + wq * 32 + 8 + (lane >> 3)) * 896 + (((lane & 7) ^ (4 + (lane >> 4))) * 16);
  char* xl = sm + K1_XOFF + wq * 16384;
#define K1_LDX(kc)                                                                                          \
  _Pragma("unroll") for (int t = 0; t < 2; ++t) {                                                           \
    glds16(xsrc0 + (size_t)t * 16 * 896 + (kc) * 128, xl + ((kc) & 3) * 4096 + t * 2048);                   \
    glds16(xsrc1 + (size_t)t * 16 * 896 + (kc) * 128, xl + ((kc) & 3) * 4096 + t * 2048 + 1024);            \
  }
#define K1_LDW(kc)                                                                                          \
  _Pragma("unroll") for (int s = 0; s < 3; ++s)                                                             \
      glds16(wsrc + (size_t)(kc) * 64 * 2688 + s * 896, sm + s * K1_WIMG + ((kc) * 64 + 16 * wq) * 64);
  const size_t tile = (size_t)h * 64 + tt * 4 + wq;
  if (w >= 4) { K1_LDX(0) K1_LDX(1) K1_LDX(2) K1_LDX(3) }
  else {
    K1_LDW(0)
    const int t2 = tid < 168 ? tid : tid - 88, t3 = tid >= 144 ? tid - 144 : tid % 112;
    touch((const char*)wqkv + (size_t)(idx * 14 + t2 / 12) * 2688 + ((t2 % 12) >> 2) * 896 + ((xcd & 1) * 3 + (t2 & 3)) * 128, sm + K1_DUMMY);
    touch((const char*)x + (size_t)((xcd >> 1) * 512 + idx * 16 + t3 / 7) * 896 + (t3 % 7) * 128, sm + K1_DUMMY);
    touch(ws + (hh ? WS_QF + tile * 2048 : WS_KVF + tile * 4096), sm + K1_DUMMY);
    K1_LDW(1) K1_LDW(2) K1_LDW(3)
  }
  TS(1);
  const unsigned lbase = (unsigned)(uintptr_t)(LDS_AS char*)sm;
  const int xsw = (r >> 1) & 7;
  unsigned xa[4], wa[4];
#pragma unroll
  for (int s = 0; s < 4; ++s) {
    const int sr = (s + wq) & 3;
    xa[s] = lbase + K1_XOFF + wq * 16384 + r * 128 + (((2 * sr + hh) ^ xsw) * 16);
    wa[s] = lbase + (w >= 4 ? 2 * K1_WIMG : 0) + (16 * sr + 8 * hh + ((lane & 15) >> 2)) * 64 + 8 * (h & 1) + 32 * ((lane >> 4) & 1) + 8 * (lane & 3);
    if (w < 4 && (lane & 19) == 19) wa[s] = lbase + ((h & 1) ? 0 : 56);
  }
  const unsigned onea = lbase + ((h & 1) ? 0 : 56) + lane * 256;
#define K1_ONES(kc) if (w == 0 && lane < 2) asm volatile("ds_write_b64 %0, %1 offset:%2" :: "v"(onea), "v"(u32x2{0x3f80u, 0u}), "i"((kc) * 4096) : "memory");
  float* tb = (float*)(sm + K1_TB);
  bf16x8 xf[3];
  s16x4 wl[3][4];
  if (w < 4) {
    f32x16 aq = {}, ak = {};
    float csq = 0.f, csk = 0.f, sq0 = 0.f, sq1 = 0.f;
#define K1_RDA(n)                                                                                            \
  {                                                                                                          \
    constexpr int kc_ = (n) >> 2, s_ = (n) & 3, b_ = (n) % 3;                                                \
    asm volatile("ds_read_b128 %0, %1 offset:%2" : "=v"(xf[b_]) : "v"(xa[s_]), "i"((kc_ & 3) * 4096));       \
    asm volatile("ds_read_b64_tr_b16 %0, %1 offset:%2" : "=v"(wl[b_][0]) : "v"(wa[s_]), "i"(kc_ * 4096));    \
    asm volatile("ds_read_b64_tr_b16 %0, %1 offset:%2" : "=v"(wl[b_][1]) : "v"(wa[s_]), "i"(kc_ * 4096 + 256)); \
    asm volatile("ds_read_b64_tr_b16 %0, %1 offset:%2" : "=v"(wl[b_][2]) : "v"(wa[s_]), "i"(K1_WIMG + kc_ * 4096)); \
    asm volatile("ds_read_b64_tr_b16 %0, %1 offset:%2" : "=v"(wl[b_][3]) : "v"(wa[s_]), "i"(K1_WIMG + kc_ * 4096 + 256)); \
  }
#define K1_WTA(c) asm volatile("s_waitcnt lgkmcnt(" #c ")" : "+v"(xf[bb]), "+v"(wl[bb][0]), "+v"(wl[bb][1]), "+v"(wl[bb][2]), "+v"(wl[bb][3]));
    WAIT_VM(12);
    K1_ONES(0)
    WAIT_LGKM0();
    BAR();
    asm volatile("" ::: "memory");
    TS(2);
    K1_RDA(0) K1_RDA(1)
    K1_LDW(4)
    auto step = [&](auto nn) __attribute__((always_inline)) {
      constexpr int n = decltype(nn)::value, kc = n >> 2, s = n & 3, bb = n % 3;
      if (n + 2 < 28) {
        if (s == 2) {
          if (kc < 3) WAIT_VM(9); else if (kc == 3) WAIT_VM(6); else if (kc == 4) WAIT_VM(3); else WAIT_VM(0);
          K1_ONES(kc + 1)
          WAIT_LGKM0(); BAR(); asm volatile("" ::: "memory");
        }
        K1_RDA(n + 2)
        if (s == 2) { if (kc == 0) { K1_LDW(5) } else if (kc == 1) { K1_LDW(6) } }
        K1_WTA(10)
      } else if (n == 26) { K1_WTA(5) } else { K1_WTA(0) }
      SB();
      const bf16x8 w0 = __builtin_shufflevector(wl[bb][0], wl[bb][1], 0, 1, 2, 3, 4, 5, 6, 7);
      const bf16x8 w1 = __builtin_shufflevector(wl[bb][2], wl[bb][3], 0, 1, 2, 3, 4, 5, 6, 7);
      aq = MFMA(w0, xf[bb], aq);
      ak = MFMA(w1, xf[bb], ak);
      const u32x4 xu = __builtin_bit_cast(u32x4, xf[bb]);
#pragma unroll
      for (int j = 0; j < 2; ++j) { const float lo = bf_lo(xu[j]), hi = bf_hi(xu[j]); sq0 = fmaf(lo, lo, sq0); sq1 = fmaf(hi, hi, sq1); }
      asm volatile("" : "+v"(sq0)); asm volatile("" : "+v"(sq1));
      if (s == 0) {
        const u32x4 u0 = __builtin_bit_cast(u32x4, w0), u1 = __builtin_bit_cast(u32x4, w1);
#pragma unroll
        for (int j = 0; j < 4; ++j) { csq = dot2(u0[j], ONES2, csq); csk = dot2(u1[j], ONES2, csk); }
      }
      if (n == 25) {
        csq = xhalf_add(csq); csk = xhalf_add(csk);
        if (hh == 0) { tb[wq * 32 + r] = csq; tb[128 + wq * 32 + r] = csk; }
      }
      SB();
      if (n == 3) TS(3);
      if (n == 15) TS(4);
    };
#define K1_S4(k) step(IC<4 * k>{}); step(IC<4 * k + 1>{}); step(IC<4 * k + 2>{}); step(IC<4 * k + 3>{});
    K1_S4(0) K1_S4(1) K1_S4(2) K1_S4(3) K1_S4(4) K1_S4(5) K1_S4(6)
    TS(5);
    {
      const float sqa = xhalf_add(sq0 + sq1);
      tb[(hh ? 384 : 512) + wq * 32 + r] = hh ? aq[12] : sqa;
    }
    WAIT_LGKM0(); BAR(); asm volatile("" ::: "memory");
    const float cq = (tb[r] + tb[32 + r]) + (tb[64 + r] + tb[96 + r]);
    const float ck = (tb[128 + r] + tb[160 + r]) + (tb[192 + r] + tb[224 + r]);
    const float mean = tb[384 + wq * 32 + r] * (1.f / 448.f);
    const float rstd = __builtin_amdgcn_rsqf(fmaxf((tb[512 + wq * 32 + r] + tb[640 + wq * 32 + r]) * (1.f / 448.f) - mean * mean, 0.f) + LN_EPS);
    const bf16x8 mf = corr_m(-mean, hh);
    aq = MFMA(corr_c(cq, hh), mf, aq);
    ak = MFMA(corr_c(ck, hh), mf, ak);
    const float rq = rstd * QSCALE;
    float sii = 0.f;
    u32x4 qo[2], ko[2];
#pragma unroll
    for (int g = 0; g < 4; ++g) {
      unsigned q0 = pk2(aq[4 * g] * rq, aq[4 * g + 1] * rq), q1 = pk2(aq[4 * g + 2] * rq, aq[4 * g + 3] * rq);
      unsigned k0 = pk2(ak[4 * g] * rstd, ak[4 * g + 1] * rstd), k1 = pk2(ak[4 * g + 2] * rstd, ak[4 * g + 3] * rstd);
      if (g == 3 && hh == 1) { q0 = 0; q1 = 0; k0 = ONES2; k1 = 0x00003f80u; }
      sii = dot2(q0, k0, sii); sii = dot2(q1, k1, sii);
      qo[g >> 1][2 * (g & 1)] = q0; qo[g >> 1][2 * (g & 1) + 1] = q1;
      ko[g >> 1][2 * (g & 1)] = k0; ko[g >> 1][2 * (g & 1) + 1] = k1;
    }
    char* qd = ws + WS_QF + tile * 2048 + lane * 16;
    char* kd = ws + WS_KVF + tile * 4096 + lane * 16;
    st16(kd, ko[0]); st16(kd + 1024, ko[1]); st16(qd, qo[0]);
    sii = xhalf_add(sii);
    if (hh == 1) { u32x2 ms = neg_split3(sii); qo[1][2] = ms[0]; qo[1][3] = ms[1]; }
    st16(qd + 1024, qo[1]);
  } else {
    f32x16 av = {};
    float csv = 0.f, sq0 = 0.f, sq1 = 0.f;
#define K1_RDB(n)                                                                                            \
  {                                                                                                          \
    constexpr int kc_ = (n) >> 2, s_ = (n) & 3, b_ = (n) % 3;                                                \
    asm volatile("ds_read_b128 %0, %1 offset:%2" : "=v"(xf[b_]) : "v"(xa[s_]), "i"((kc_ & 3) * 4096));       \
    asm volatile("ds_read_b64_tr_b16 %0, %1 offset:%2" : "=v"(wl[b_][0]) : "v"(wa[s_]), "i"(kc_ * 4096));    \
    asm volatile("ds_read_b64_tr_b16 %0, %1 offset:%2" : "=v"(wl[b_][1]) : "v"(wa[s_]), "i"(kc_ * 4096 + 256)); \
  }
#define K1_WTB(c) asm volatile("s_waitcnt lgkmcnt(" #c ")" : "+v"(xf[bb]), "+v"(wl[bb][0]), "+v"(wl[bb][1]));
    WAIT_VM(12);
    BAR();
    asm volatile("" ::: "memory");
    TS(2);
    K1_RDB(0) K1_RDB(1)
    auto step = [&](auto nn) __attribute__((always_inline)) {
      constexpr int n = decltype(nn)::value, kc = n >> 2, s = n & 3, bb = n % 3;
      if (n + 2 < 28) {
        if (s == 2) {
          WAIT_LGKM0();
          if (kc < 4) WAIT_VM(8); else if (kc == 4) WAIT_VM(4); else WAIT_VM(0);
          BAR();
          asm volatile("" ::: "memory");
        }
        K1_RDB(n + 2)
        if (s == 2) { if (kc == 0) { K1_LDX(4) } else if (kc == 1) { K1_LDX(5) } else if (kc == 2) { K1_LDX(6) } }
        K1_WTB(6)
      } else if (n == 26) { K1_WTB(3) } else { K1_WTB(0) }
      SB();
      const bf16x8 w2 = __builtin_shufflevector(wl[bb][0], wl[bb][1], 0, 1, 2, 3, 4, 5, 6, 7);
      av = MFMA(xf[bb], w2, av);
      const u32x4 xu = __builtin_bit_cast(u32x4, xf[bb]);
#pragma unroll
      for (int j = 2; j < 4; ++j) { const float lo = bf_lo(xu[j]), hi = bf_hi(xu[j]); sq0 = fmaf(lo, lo, sq0); sq1 = fmaf(hi, hi, sq1); }
      asm volatile("" : "+v"(sq0)); asm volatile("" : "+v"(sq1));
      if (s == 0) {
        const u32x4 u2 = __builtin_bit_cast(u32x4, w2);
#pragma unroll
        for (int j = 0; j < 4; ++j) csv = dot2(u2[j], ONES2, csv);
      }
      SB();
      if (n == 3) TS(3);
      if (n == 15) TS(4);
    };
    K1_S4(0) K1_S4(1) K1_S4(2) K1_S4(3) K1_S4(4) K1_S4(5) K1_S4(6)
    TS(5);
    {
      const float sqb = xhalf_add(sq0 + sq1);
      csv = xhalf_add(csv);
      tb[(hh ? 256 : 640) + wq * 32 + r] = hh ? csv : sqb;
    }
    WAIT_LGKM0(); BAR(); asm volatile("" ::: "memory");
    const float cv = (tb[256 + r] + tb[288 + r]) + (tb[320 + r] + tb[352 + r]);
    const float mean = tb[384 + wq * 32 + r] * (1.f / 448.f);
    const float rstd = __builtin_amdgcn_rsqf(fmaxf((tb[512 + wq * 32 + r] + tb[640 + wq * 32 + r]) * (1.f / 448.f) - mean * mean, 0.f) + LN_EPS);
    tb[768 + wq * 32 + r] = rstd;
    f32x4 rs[4];
#pragma unroll
    for (int g = 0; g < 4; ++g) rs[g] = *(const f32x4*)(tb + 768 + wq * 32 + 8 * g + 4 * hh);
    av = MFMA(corr_m(-mean, hh), corr_c(cv, hh), av);
    u32x4 vo[2];
#pragma unroll
    for (int g = 0; g < 4; ++g) {
      unsigned v0 = pk2(av[4 * g] * rs[g][0], av[4 * g + 1] * rs[g][1]), v1 = pk2(av[4 * g + 2] * rs[g][2], av[4 * g + 3] * rs[g][3]);
      if (r >= 28) v0 = v1 = (r == 28) ? ONES2 : 0u;
      vo[g >> 1][2 * (g & 1)] = v0; vo[g >> 1][2 * (g & 1) + 1] = v1;
    }
    char* kd = ws + WS_KVF + tile * 4096 + lane * 16;
    st16(kd + 2048, vo[0]); st16(kd + 3072, vo[1]);
  }
  TS_OUT(0);
}

constexpr int K2_QL = 8 * 16384;
constexpr int K2_LS = K2_QL + 8192;
constexpr int K2_LDS = K2_LS + 4096;

__global__ __launch_bounds__(512, 2) void k2_attn(const uint16_t* __restrict__ wo, const uint16_t* __restrict__ wg,
                                                  const uint16_t* __restrict__ wu, const uint16_t* __restrict__ wd,
                                                  char* __restrict__ ws) {
  extern __shared__ __attribute__((aligned(16))) char sm[];
  const int tid = threadIdx.x, lane = tid & 63, w = __builtin_amdgcn_readfirstlane(tid >> 6);
  const int b = blockIdx.x, xcd = b & 7, idx = b >> 3;
  const int h = 2 * xcd + (idx & 1), qt = idx >> 1;
  const int r = lane & 31, hh = lane >> 5;
  TS_DECL; TS(0);
  const unsigned l16 = lane * 16;
  const unsigned lbase = (unsigned)(uintptr_t)(LDS_AS char*)sm;
  glds16o<0>(ws + WS_QF + ((size_t)h * 64 + qt * 4) * 2048 + w * 1024, l16, sm + K2_QL + w * 1024);
  const char* kvsrc = ws + WS_KVF + ((size_t)h * 64 + w * 8) * 4096;
  bf16x8 kv[8][4];
#pragma unroll
  for (int t = 0; t < 8; ++t)
#pragma unroll
    for (int c = 0; c < 4; ++c)
      asm volatile("global_load_dwordx4 %0, %1, %2 offset:%3" : "=v"(kv[t][c]) : "v"(l16), "s"(kvsrc + t * 4096), "i"(c * 1024) : "memory");
  SB();
  char* wdst[3];
  {
    const int gw = b * 8 + w;
#pragma unroll
    for (int it = 0; it < 3; ++it) {
      int f = gw + it * 2048;
      if (f >= 5096) f = gw;
      const uint16_t* src;
      if (f < 3136) {
        const int cl = f & 3, t = f >> 2, ks = t % 28, c = (t / 28) * 4 + cl;
        src = ((lane & 2) ? wu : wg) + (size_t)(16 * ks + (lane >> 2)) * 1792 + 16 * c + 8 * (lane & 1);
        wdst[it] = ws + WS_WGF + ((size_t)c * 28 + ks) * 1024;
      } else if (f < 3136 + 1568) {
        const int g = f - 3136, nl = g & 1, t = g >> 1, ks = t % 112, nb = (t / 112) * 2 + nl;
        src = wd + (size_t)(16 * ks + (lane >> 2)) * 448 + 32 * nb + 8 * (lane & 3);
        wdst[it] = ws + WS_WDF + ((size_t)nb * 112 + ks) * 1024;
      } else {
        const int g = f - 4704, nl = g & 1, t = g >> 1, ks = t % 28, nb = (t / 28) * 2 + nl;
        src = wo + (size_t)(16 * ks + (lane >> 2)) * 448 + 32 * nb + 8 * (lane & 3);
        wdst[it] = ws + WS_WOF + ((size_t)nb * 28 + ks) * 1024;
      }
      glds16(src, sm + w * 16384 + 12288 + it * 1024);
    }
  }
  TS(1);
  WAIT_VM(35);
  BAR();
  asm volatile("" ::: "memory");
  TS(2);
  bf16x8 q[4][2];
  const unsigned qa = lbase + K2_QL + l16;
#define K2_RDQ(i)                                                                                            \
  asm volatile("ds_read_b128 %0, %1 offset:%2" : "=v"(q[i][0]) : "v"(qa), "i"((i) * 2048));                  \
  asm volatile("ds_read_b128 %0, %1 offset:%2" : "=v"(q[i][1]) : "v"(qa), "i"((i) * 2048 + 1024));
#define K2_WTQ(i) asm volatile("s_waitcnt lgkmcnt(0)" : "+v"(q[i][0]), "+v"(q[i][1]));
  K2_RDQ(0) K2_RDQ(1)
  K2_WTQ(0) K2_WTQ(1)
  float* ls = (float*)(sm + K2_LS);
  auto put = [&](const f32x16& o, int i) __attribute__((always_inline)) {
#pragma unroll
    for (int g = 0; g < 4; ++g)
      *(f32x4*)(sm + w * 16384 + (i * 4 + g) * 1024 + l16) = f32x4{o[4 * g], o[4 * g + 1], o[4 * g + 2], o[4 * g + 3]};
    if (hh == 1) ls[w * 128 + i * 32 + r] = o[12];
  };
  f32x16 o[2], sc[2];
  asm volatile("s_waitcnt vmcnt(33)" : "+v"(kv[0][0]), "+v"(kv[0][1]));
  sc[0] = MFMA(kv[0][0], q[0][0], (f32x16{}));
  sc[0] = MFMA(kv[0][1], q[0][1], sc[0]);
  auto tile = [&](auto nn) __attribute__((always_inline)) {
    constexpr int n = decltype(nn)::value, m = n + 1;
    constexpr int t = n < 16 ? n >> 1 : n & 7, i = n < 16 ? n & 1 : n >> 3;
    constexpr int tn = m < 16 ? m >> 1 : m & 7, in = m < 16 ? m & 1 : (m >> 3) & 3;
    f32x16& s = sc[n & 1];
    f32x16& sn = sc[m & 1];
    f32x16& oo = o[i & 1];
    if (n == 13) { K2_RDQ(2) }
    if (n == 21) { K2_RDQ(3) }
    if (n == 15) { K2_WTQ(2) }
    if (n == 23) { K2_WTQ(3) }
    if (n == 0) asm volatile("s_waitcnt vmcnt(31)" : "+v"(kv[0][2]), "+v"(kv[0][3]));
    if (n < 15 && (n & 1))
      asm volatile("s_waitcnt vmcnt(%4)" : "+v"(kv[tn][0]), "+v"(kv[tn][1]), "+v"(kv[tn][2]), "+v"(kv[tn][3]) : "i"((7 - tn) * 4 + 3));
    SB();
    if (m < 32) sn = MFMA(kv[tn][0], q[in][0], (f32x16{}));
    SB();
#pragma unroll
    for (int e = 0; e < 4; ++e) s[e] = __builtin_amdgcn_exp2f(s[e]);
    SB();
    if (m < 32) sn = MFMA(kv[tn][1], q[in][1], sn);
    SB();
#pragma unroll
    for (int e = 4; e < 8; ++e) s[e] = __builtin_amdgcn_exp2f(s[e]);
    const bf16x8 p0 = pack8(s, 0);
    SB();
    if (t == 0) oo = MFMA(kv[t][2], p0, (f32x16{})); else oo = MFMA(kv[t][2], p0, oo);
    SB();
#pragma unroll
    for (int e = 8; e < 16; ++e) s[e] = __builtin_amdgcn_exp2f(s[e]);
    const bf16x8 p1 = pack8(s, 1);
    SB();
    oo = MFMA(kv[t][3], p1, oo);
    SB();
    if (n == 15) put(o[0], 0);
    if (n == 16) put(o[1], 1);
    if (n == 24) put(o[0], 2);
    if (n == 31) put(o[1], 3);
    if (n == 18) {
      WAIT_VM(0);
      const unsigned wa = lbase + w * 16384 + 12288 + (4 * hh + ((lane & 15) >> 2)) * 64 + 32 * ((lane >> 4) & 1) + 8 * (lane & 3);
      s16x4 wl[3][2];
#pragma unroll
      for (int it = 0; it < 3; ++it) {
        asm volatile("ds_read_b64_tr_b16 %0, %1 offset:%2" : "=v"(wl[it][0]) : "v"(wa), "i"(it * 1024));
        asm volatile("ds_read_b64_tr_b16 %0, %1 offset:%2" : "=v"(wl[it][1]) : "v"(wa), "i"(it * 1024 + 512));
      }
      asm volatile("s_waitcnt lgkmcnt(0)" : "+v"(wl[0][0]), "+v"(wl[0][1]), "+v"(wl[1][0]), "+v"(wl[1][1]), "+v"(wl[2][0]), "+v"(wl[2][1]) :: "memory");
#pragma unroll
      for (int it = 0; it < 3; ++it)
        if (it < 2 || b * 8 + w + 4096 < 5096)
          st16(wdst[it] + l16, __builtin_bit_cast(u32x4, __builtin_shufflevector(wl[it][0], wl[it][1], 0, 1, 2, 3, 4, 5, 6, 7)));
    }
    SB();
    if (n == 1) TS(3);
    if (n == 15) TS(4);
  };
#define K2_T4(k) tile(IC<4 * k>{}); tile(IC<4 * k + 1>{}); tile(IC<4 * k + 2>{}); tile(IC<4 * k + 3>{});
  K2_T4(0) K2_T4(1) K2_T4(2) K2_T4(3) K2_T4(4) K2_T4(5) K2_T4(6) K2_T4(7)
  TS(5);
  const int oi = w >> 1, og = 2 * (w & 1);
  f32x4 res[2];
  bool bad;
  auto gather = [&]() __attribute__((always_inline)) {
    __syncthreads();
    float l = 0.f;
    f32x2 la = {};
    res[0] = f32x4{}; res[1] = f32x4{};
#pragma unroll
    for (int ww = 0; ww < 8; ++ww) {
      res[0] += *(const f32x4*)(sm + ww * 16384 + (oi * 4 + og) * 1024 + l16);
      res[1] += *(const f32x4*)(sm + ww * 16384 + (oi * 4 + og + 1) * 1024 + l16);
      l += ls[ww * 128 + oi * 32 + r];
      la += *(const f32x2*)(ls + ww * 128 + 2 * lane);
    }
    const float rl = 1.f / l;
    res[0] *= rl; res[1] *= rl;
    bad = __any(!(fmaxf(la[0], la[1]) < 1.0e9f) || !(la[0] == la[0]) || !(la[1] == la[1]));
  };
  gather();
  TS(6);
  if (bad) {
    __syncthreads();
    u32x4 qq[4][2];
#pragma unroll
    for (int i = 0; i < 4; ++i) {
      qq[i][0] = *(const u32x4*)(sm + K2_QL + i * 2048 + l16);
      qq[i][1] = *(const u32x4*)(sm + K2_QL + i * 2048 + 1024 + l16);
      if (hh == 1) { qq[i][1][2] = 0; qq[i][1][3] = 0; }
    }
    float m[4] = {-INFINITY, -INFINITY, -INFINITY, -INFINITY};
#pragma unroll 1
    for (int t = 0; t < 8; ++t) {
      const bf16x8 k0 = *(const bf16x8*)(kvsrc + t * 4096 + l16), k1 = *(const bf16x8*)(kvsrc + t * 4096 + 1024 + l16);
#pragma unroll
      for (int i = 0; i < 4; ++i) {
        f32x16 s = {};
        s = MFMA(k0, __builtin_bit_cast(bf16x8, qq[i][0]), s);
        s = MFMA(k1, __builtin_bit_cast(bf16x8, qq[i][1]), s);
#pragma unroll
        for (int e = 0; e < 16; ++e) m[i] = fmaxf(m[i], s[e]);
      }
    }
#pragma unroll
    for (int i = 0; i < 4; ++i) { m[i] = xhalf_max(m[i]); if (hh == 0) ls[w * 128 + i * 32 + r] = m[i]; }
    __syncthreads();
#pragma unroll
    for (int i = 0; i < 4; ++i) {
      float mm = -INFINITY;
#pragma unroll
      for (int ww = 0; ww < 8; ++ww) mm = fmaxf(mm, ls[ww * 128 + i * 32 + r]);
      const u32x2 ms = neg_split3(mm);
      if (hh == 1) { qq[i][1][2] = ms[0]; qq[i][1][3] = ms[1]; }
    }
    __syncthreads();
    f32x16 of[4] = {};
#pragma unroll 1
    for (int t = 0; t < 8; ++t) {
      const bf16x8 k0 = *(const bf16x8*)(kvsrc + t * 4096 + l16), k1 = *(const bf16x8*)(kvsrc + t * 4096 + 1024 + l16);
      const bf16x8 v0 = *(const bf16x8*)(kvsrc + t * 4096 + 2048 + l16), v1 = *(const bf16x8*)(kvsrc + t * 4096 + 3072 + l16);
#pragma unroll
      for (int i = 0; i < 4; ++i) {
        f32x16 s = {};
        s = MFMA(k0, __builtin_bit_cast(bf16x8, qq[i][0]), s);
        s = MFMA(k1, __builtin_bit_cast(bf16x8, qq[i][1]), s);
#pragma unroll
        for (int e = 0; e < 16; ++e) s[e] = __builtin_amdgcn_exp2f(s[e]);
        of[i] = MFMA(v0, pack8(s, 0), of[i]);
        of[i] = MFMA(v1, pack8(s, 1), of[i]);
      }
    }
#pragma unroll
    for (int i = 0; i < 4; ++i) put(of[i], i);
    gather();
  }
#pragma unroll
  for (int c = 0; c < 2; ++c) {
    const int gl = 2 * (og + c) + hh;
    const int gi = 7 * h + gl;
    if (gl < 7) {
      char* od = ws + WS_OF + ((size_t)(qt * 4 + oi) * 28 + (gi >> 2)) * 1024 + ((gi & 1) * 32 + r) * 16 + ((gi >> 1) & 1) * 8;
      *(u32x2*)od = u32x2{pk2(res[c][0], res[c][1]), pk2(res[c][2], res[c][3])};
    }
  }
  TS_OUT(1);
}

template <int T>
DI void red_put(const f32x16& a, char* sm, int w, int lane) {
#pragma unroll
  for (int g = 0; g < 4; ++g)
    *(f32x4*)(sm + ((w * 4 + T) * 4 + g) * 1024 + lane * 16) = f32x4{a[4 * g], a[4 * g + 1], a[4 * g + 2], a[4 * g + 3]};
}
DI f32x16 reduce4(f32x16 (&acc)[2][2], char* sm, int w, int lane) {
  f32x16 o;
  if (w != 0) red_put<0>(acc[0][0], sm, w, lane); else o = acc[0][0];
  if (w != 1) red_put<1>(acc[0][1], sm, w, lane); else o = acc[0][1];
  if (w != 2) red_put<2>(acc[1][0], sm, w, lane); else o = acc[1][0];
  if (w != 3) red_put<3>(acc[1][1], sm, w, lane); else o = acc[1][1];
  __syncthreads();
  f32x4 p[3][4];
#pragma unroll
  for (int i = 0; i < 3; ++i) {
    const int ww = (w + 1 + i) & 3;
#pragma unroll
    for (int g = 0; g < 4; ++g) p[i][g] = *(const f32x4*)(sm + ((ww * 4 + w) * 4 + g) * 1024 + lane * 16);
  }
#pragma unroll
  for (int g = 0; g < 4; ++g)
#pragma unroll
    for (int j = 0; j < 4; ++j) o[4 * g + j] = (o[4 * g + j] + p[0][g][j]) + (p[1][g][j] + p[2][g][j]);
  return o;
}

__global__ __launch_bounds__(256, 1) void k3_oproj(const uint16_t* __restrict__ x, char* __restrict__ ws) {
  extern __shared__ __attribute__((aligned(16))) char sm[];
  const int tid = threadIdx.x, lane = tid & 63, w = __builtin_amdgcn_readfirstlane(tid >> 6);
  const int b = blockIdx.x, r = lane & 31, hh = lane >> 5;
  TS_DECL; TS(0);
  if (b >= 224) {
    const int c = (b - 224) * 4 + w;
    const char* src = ws + WS_WGF + (size_t)c * 28 * 1024 + lane * 16;
    u32x4 v[28];
#pragma unroll
    for (int i = 0; i < 28; ++i) v[i] = *(const u32x4*)(src + i * 1024);
    SB();
    float s0 = 0.f, s1 = 0.f;
#pragma unroll
    for (int i = 0; i < 28; ++i) { s0 = dot2(v[i][0], ONES2, s0); s1 = dot2(v[i][1], ONES2, s1); s0 = dot2(v[i][2], ONES2, s0); s1 = dot2(v[i][3], ONES2, s1); }
    const float s = xhalf_add(s0 + s1);
    if (hh == 0) ((float*)(ws + WS_CS))[c * 32 + r] = s;
    TS_OUT(2);
    return;
  }
  const int xcd = b & 7, idx = b >> 3, t64 = 4 * xcd + (idx & 3), nb = idx >> 2;
  const char* osrc = ws + WS_OF + ((size_t)(2 * t64) * 28 + 7 * w) * 1024 + lane * 16;
  const char* wsrc = ws + WS_WOF + ((size_t)(2 * nb) * 28 + 7 * w) * 1024 + lane * 16;
  bf16x8 of[7][2], wf[7][2];
#pragma unroll
  for (int s = 0; s < 7; ++s) {
    of[s][0] = *(const bf16x8*)(osrc + s * 1024); wf[s][0] = *(const bf16x8*)(wsrc + s * 1024);
    of[s][1] = *(const bf16x8*)(osrc + (28 + s) * 1024); wf[s][1] = *(const bf16x8*)(wsrc + (28 + s) * 1024);
  }
  const int tt32 = 2 * t64 + (w & 1), n32 = 2 * nb + (w >> 1);
  const uint16_t* xs = x + (size_t)(tt32 * 32 + r) * 448 + n32 * 32 + 4 * hh;
  u32x2 xr[4];
#pragma unroll
  for (int g = 0; g < 4; ++g) xr[g] = *(const u32x2*)(xs + 8 * g);
  SB();
  TS(1);
  f32x16 acc[2][2] = {};
#pragma unroll
  for (int s = 0; s < 7; ++s) {
#pragma unroll
    for (int c = 0; c < 2; ++c)
#pragma unroll
      for (int a = 0; a < 2; ++a) acc[c][a] = MFMA(wf[s][c], of[s][a], acc[c][a]);
    SB();
  }
  TS(2);
  f32x16 o = reduce4(acc, sm, w, lane);
  TS(3);
  float s1 = 0.f, s2 = 0.f;
  u32x4 ho[2];
#pragma unroll
  for (int g = 0; g < 4; ++g) {
    const float h0 = bf_lo(xr[g][0]) + rbf(o[4 * g]), h1 = bf_hi(xr[g][0]) + rbf(o[4 * g + 1]);
    const float h2 = bf_lo(xr[g][1]) + rbf(o[4 * g + 2]), h3 = bf_hi(xr[g][1]) + rbf(o[4 * g + 3]);
    const unsigned p0 = pk2(h0, h1), p1 = pk2(h2, h3);
    s1 = dot2(p0, ONES2, s1); s1 = dot2(p1, ONES2, s1);
    s2 = dot2(p0, p0, s2); s2 = dot2(p1, p1, s2);
    ho[g >> 1][2 * (g & 1)] = p0; ho[g >> 1][2 * (g & 1) + 1] = p1;
  }
  char* hd = ws + WS_HF + ((size_t)tt32 * 28 + 2 * n32) * 1024 + lane * 16;
  *(u32x4*)hd = ho[0]; *(u32x4*)(hd + 1024) = ho[1];
  s1 = xhalf_add(s1); s2 = xhalf_add(s2);
  if (hh == 0) *(f32x2*)(ws + WS_ST + ((size_t)n32 * 2048 + tt32 * 32 + r) * 8) = f32x2{s1, s2};
  TS_OUT(2);
}

constexpr int K4_CH = 28 * 1024;
constexpr int K4_ST = 5 * K4_CH;
constexpr int K4_CS = K4_ST + 14 * 1024;
constexpr int K4_LDS = K4_CS + 1024;

__global__ __launch_bounds__(256, 2) void k4_ffn_up(char* __restrict__ ws) {
  extern __shared__ __attribute__((aligned(16))) char sm[];
  const int tid = threadIdx.x, lane = tid & 63, w = __builtin_amdgcn_readfirstlane(tid >> 6);
  const int b = blockIdx.x, xcd = b & 7, idx = b >> 3;
  const int tt = (xcd & 1) * 8 + (idx & 7), cg = (xcd >> 1) * 4 + (idx >> 3);
  const int r = lane & 31, hh = lane >> 5;
  TS_DECL; TS(0);
  const char* wsrc = ws + WS_WGF + (size_t)cg * 7 * K4_CH + w * 7168;
  const char* hsrc = ws + WS_HF + ((size_t)(tt * 4 + w) * 28) * 1024;
  const unsigned l16 = lane * 16;
#define K4_LDW(c)                                                                                            \
  {                                                                                                          \
    const char* g_ = wsrc + (c) * K4_CH; char* l_ = sm + ((c) % 5) * K4_CH + w * 7168;                       \
    glds16o<0>(g_, l16, l_); glds16o<1024>(g_, l16, l_); glds16o<2048>(g_, l16, l_); glds16o<3072>(g_, l16, l_); \
    glds16o<0>(g_ + 4096, l16, l_ + 4096); glds16o<1024>(g_ + 4096, l16, l_ + 4096); glds16o<2048>(g_ + 4096, l16, l_ + 4096); \
  }
  K4_LDW(0)
  bf16x8 hf[28];
#pragma unroll
  for (int i = 0; i < 28; ++i)
    asm volatile("global_load_dwordx4 %0, %1, %2 offset:%3" : "=v"(hf[i]) : "v"(l16), "s"(hsrc + (i >> 2) * 4096), "i"((i & 3) * 1024) : "memory");
  {
    const char* st = ws + WS_ST + (size_t)tt * 1024 + lane * 16;
#pragma unroll
    for (int i = 0; i < 4; ++i) {
      const int n = w * 4 + i;
      const char* src = n < 14 ? st + (size_t)n * 16384 : ws + WS_CS + cg * 896 + lane * 16;
      glds16(src, sm + K4_ST + (n < 14 ? n : 14) * 1024);
    }
  }
  K4_LDW(1) K4_LDW(2) K4_LDW(3)
  TS(1);
  asm volatile("s_waitcnt vmcnt(21)"
               : "+v"(hf[0]), "+v"(hf[1]), "+v"(hf[2]), "+v"(hf[3]), "+v"(hf[4]), "+v"(hf[5]), "+v"(hf[6]), "+v"(hf[7]),
                 "+v"(hf[8]), "+v"(hf[9]), "+v"(hf[10]), "+v"(hf[11]), "+v"(hf[12]), "+v"(hf[13])
               :: "memory");
  asm volatile(""
               : "+v"(hf[14]), "+v"(hf[15]), "+v"(hf[16]), "+v"(hf[17]), "+v"(hf[18]), "+v"(hf[19]), "+v"(hf[20]), "+v"(hf[21]),
                 "+v"(hf[22]), "+v"(hf[23]), "+v"(hf[24]), "+v"(hf[25]), "+v"(hf[26]), "+v"(hf[27])
               :: "memory");
  BAR();
  asm volatile("" ::: "memory");
  TS(2);
  constexpr int PD = 6;
  bf16x8 wf[PD];
  const unsigned lb = (unsigned)(uintptr_t)(LDS_AS char*)sm + l16;
  const unsigned lb0 = lb, lb1 = lb + 2 * K4_CH, lb2 = lb + 4 * K4_CH;
  const unsigned lcs = (unsigned)(uintptr_t)(LDS_AS char*)sm + K4_CS + 16 * hh;
  const unsigned lst = (unsigned)(uintptr_t)(LDS_AS char*)sm + K4_ST + hh * 7168 + (w * 32 + r) * 8;
#define K4_RD(dst, c, ks)                                                                                    \
  {                                                                                                          \
    constexpr int sl_ = (c) % 5;                                                                             \
    asm volatile("ds_read_b128 %0, %1 offset:%2" : "=v"(dst) : "v"(sl_ < 2 ? lb0 : sl_ < 4 ? lb1 : lb2), "i"((sl_ & 1) * K4_CH + (ks) * 1024)); \
  }
  f32x2 st[7];
#pragma unroll
  for (int i = 0; i < PD; ++i) K4_RD(wf[i], 0, i)
#pragma unroll
  for (int i = 0; i < 7; ++i) asm volatile("ds_read_b64 %0, %1 offset:%2" : "=v"(st[i]) : "v"(lst), "i"(i * 1024));
  SB();
  K4_LDW(4)
  char* adst = ws + WS_AF + ((size_t)(tt * 4 + w) * 112 + cg * 7) * 1024 + lane * 16;
  constexpr float GC1 = -2.f * 0.7978845608028654f * 1.4426950408889634f, GC3 = GC1 * 0.044715f;
  float rstd = 0.f, nmr = 0.f;
  f32x16 acc[2];
  f32x4 cs[2][4];
  float gt[8], up[8], ez[8];
  unsigned ap[4];
  asm volatile("s_waitcnt lgkmcnt(11)" : "+v"(wf[0]), "+v"(wf[1]));
  SB();
  auto chunk = [&](auto cc) __attribute__((always_inline)) {
    constexpr int c = decltype(cc)::value;
#pragma unroll
    for (int ks = 0; ks < 28; ++ks) {
      if (c < 7) {
        constexpr int g0 = c * 28;
        if (ks == 0) acc[c & 1] = MFMA(wf[(g0 + ks) % PD], hf[ks], (f32x16{}));
        else acc[c & 1] = MFMA(wf[(g0 + ks) % PD], hf[ks], acc[c & 1]);
        SB();
        if (ks + PD == 28 && c < 6) {
          if (c == 0) WAIT_VM(21);
          else if (c == 1) WAIT_VM(21);
          else if (c == 2) WAIT_VM(22);
          else if (c == 3) WAIT_VM(16);
          else if (c == 4) WAIT_VM(10);
          else WAIT_VM(4);
          if (c < 2) WAIT_LGKM0();
          BAR();
          asm volatile("" ::: "memory");
        }
        if (ks + PD < 28) K4_RD(wf[(g0 + ks) % PD], c, ks + PD)
        else if (c < 6) K4_RD(wf[(g0 + ks) % PD], c + 1, ks + PD - 28)
        if (ks + PD == 28) { if (c == 0) { K4_LDW(5) } else if (c == 1) { K4_LDW(6) } }
        if (ks == 9) {
#pragma unroll
          for (int g = 0; g < 4; ++g) asm volatile("ds_read_b128 %0, %1 offset:%2" : "=v"(cs[c & 1][g]) : "v"(lcs), "i"(c * 128 + 32 * g));
        }
        if ((ks & 1) && !(c == 6 && ks == 27)) {
          const int kn = ks + 1;
#define K4_WT(n) asm volatile("s_waitcnt lgkmcnt(" #n ")" : "+v"(wf[(g0 + ks + 1) % PD]), "+v"(wf[(g0 + ks + 2) % PD]))
          if (c == 0 && kn < 6) K4_WT(11);
          else if (kn >= 10 && kn < 16) K4_WT(8);
          else if (c == 6 && kn == 24) K4_WT(2);
          else if (c == 6 && kn == 26) K4_WT(0);
          else K4_WT(4);
          SB();
        }
        if (ks == 16) asm volatile("" : "+v"(cs[c & 1][0]), "+v"(cs[c & 1][1]), "+v"(cs[c & 1][2]), "+v"(cs[c & 1][3]));
      }
      if (c == 0) {
        if (ks == 6) {
          asm volatile("" : "+v"(st[0]), "+v"(st[1]), "+v"(st[2]), "+v"(st[3]), "+v"(st[4]), "+v"(st[5]), "+v"(st[6]));
          float s1 = ((st[0][0] + st[1][0]) + (st[2][0] + st[3][0])) + ((st[4][0] + st[5][0]) + st[6][0]);
          float s2 = ((st[0][1] + st[1][1]) + (st[2][1] + st[3][1])) + ((st[4][1] + st[5][1]) + st[6][1]);
          s1 = xhalf_add(s1); s2 = xhalf_add(s2);
          const float mean = s1 * (1.f / 448.f);
          rstd = __builtin_amdgcn_rsqf(fmaxf(s2 * (1.f / 448.f) - mean * mean, 0.f) + LN_EPS);
          nmr = -mean * rstd;
        }
      } else {
        const f32x16& a = acc[(c - 1) & 1];
        const f32x4(&cv)[4] = cs[(c - 1) & 1];
        if (ks >= 2 && ks < 26) {
          const int e = (ks - 2) / 3, ph = (ks - 2) % 3;
          if (ph == 0) {
            const float tg = nmr * cv[e >> 2][e & 3], tu = nmr * cv[2 + (e >> 2)][e & 3];
            gt[e] = fmaf(a[e], rstd, tg);
            up[e] = fmaf(a[8 + e], rstd, tu);
            if (c < 7) { asm volatile("" : "+v"(gt[e])); asm volatile("" : "+v"(up[e])); }
          } else if (ph == 1) {
            ez[e] = __builtin_amdgcn_exp2f(gt[e] * fmaf(gt[e] * gt[e], GC3, GC1));
            up[e] = gt[e] * up[e];
            if (c < 7) { asm volatile("" : "+v"(ez[e])); asm volatile("" : "+v"(up[e])); }
          } else {
            up[e] = up[e] * __builtin_amdgcn_rcpf(1.f + ez[e]);
            if (c < 7) asm volatile("" : "+v"(up[e]));
            if (e & 1) { ap[e >> 1] = pk2(up[e - 1], up[e]); if (c < 7) asm volatile("" : "+v"(ap[e >> 1])); }
          }
        }
        if (ks == 26) st16(adst + (c - 1) * 1024, u32x4{ap[0], ap[1], ap[2], ap[3]});
      }
      if (c < 7) SB();
    }
    if (c == 0) TS(3);
    if (c == 3) { TS(4); CY0(); }
    if (c == 6) { TS(5); CY1(); }
  };
  chunk(IC<0>{}); chunk(IC<1>{}); chunk(IC<2>{}); chunk(IC<3>{}); chunk(IC<4>{}); chunk(IC<5>{}); chunk(IC<6>{}); chunk(IC<7>{});
  TS_OUT(3);
}

__global__ __launch_bounds__(256, 1) void k5_ffn_down(char* __restrict__ ws, uint16_t* __restrict__ out) {
  extern __shared__ __attribute__((aligned(16))) char sm[];
  const int tid = threadIdx.x, lane = tid & 63, w = __builtin_amdgcn_readfirstlane(tid >> 6);
  const int b = blockIdx.x, r = lane & 31, hh = lane >> 5;
  TS_DECL; TS(0);
  const int xcd = b & 7, idx = b >> 3, t64 = 4 * xcd + (idx & 3), nb = idx >> 2;
  const char* asrc = ws + WS_AF + ((size_t)(2 * t64) * 112 + 28 * w) * 1024 + lane * 16;
  const char* wsrc = ws + WS_WDF + ((size_t)(2 * nb) * 112 + 28 * w) * 1024 + lane * 16;
  constexpr int P = 14;
  bf16x8 af[P][2], wf[P][2];
#pragma unroll
  for (int s = 0; s < P; ++s) {
    af[s][0] = *(const bf16x8*)(asrc + s * 1024); wf[s][0] = *(const bf16x8*)(wsrc + s * 1024);
    af[s][1] = *(const bf16x8*)(asrc + (112 + s) * 1024); wf[s][1] = *(const bf16x8*)(wsrc + (112 + s) * 1024);
  }
  SB();
  TS(1);
  const int tt32 = 2 * t64 + (w & 1), n32 = 2 * nb + (w >> 1);
  const char* hs = ws + WS_HF + ((size_t)tt32 * 28 + 2 * n32) * 1024 + lane * 16;
  u32x4 h0, h1;
  f32x16 acc[2][2] = {};
#pragma unroll
  for (int s = 0; s < 28; ++s) {
#pragma unroll
    for (int c = 0; c < 2; ++c)
#pragma unroll
      for (int a = 0; a < 2; ++a) acc[c][a] = MFMA(wf[s % P][c], af[s % P][a], acc[c][a]);
    SB();
    if (s + P < 28) {
      af[s % P][0] = *(const bf16x8*)(asrc + (s + P) * 1024); wf[s % P][0] = *(const bf16x8*)(wsrc + (s + P) * 1024);
      af[s % P][1] = *(const bf16x8*)(asrc + (112 + s + P) * 1024); wf[s % P][1] = *(const bf16x8*)(wsrc + (112 + s + P) * 1024);
    }
    if (s + P == 28) { h0 = *(const u32x4*)hs; h1 = *(const u32x4*)(hs + 1024); }
    SB();
    if (s == 0) TS(2);
    if (s == 13) TS(3);
  }
  TS(4);
  f32x16 o = reduce4(acc, sm, w, lane);
  TS(5);
  unsigned pq[4][2];
#pragma unroll
  for (int g = 0; g < 4; ++g) {
    const unsigned ha = g < 2 ? h0[2 * (g & 1)] : h1[2 * (g & 1)], hb = g < 2 ? h0[2 * (g & 1) + 1] : h1[2 * (g & 1) + 1];
    pq[g][0] = pk2(bf_lo(ha) + rbf(o[4 * g]), bf_hi(ha) + rbf(o[4 * g + 1]));
    pq[g][1] = pk2(bf_lo(hb) + rbf(o[4 * g + 2]), bf_hi(hb) + rbf(o[4 * g + 3]));
  }
  uint16_t* od = out + (size_t)(tt32 * 32 + r) * 448 + n32 * 32 + 8 * hh;
#pragma unroll
  for (int g = 0; g < 4; g += 2) {
    auto r0 = __builtin_amdgcn_permlane32_swap(pq[g][0], pq[g + 1][0], false, false);
    auto r1 = __builtin_amdgcn_permlane32_swap(pq[g][1], pq[g + 1][1], false, false);
    *(u32x4*)(od + 8 * g) = u32x4{r0[0], r1[0], r0[1], r1[1]};
  }
  TS_OUT(4);
}

extern "C" void kernel_launch(void* const* d_in, const int* in_sizes, int n_in, void* d_out, int out_size, void* d_ws,
                              size_t ws_size, hipStream_t stream) {
  const uint16_t* x = (const uint16_t*)d_in[0];
  const uint16_t* wqkv = (const uint16_t*)d_in[1];
  const uint16_t* wo = (const uint16_t*)d_in[2];
  const uint16_t* wg = (const uint16_t*)d_in[4];
  const uint16_t* wu = (const uint16_t*)d_in[5];
  const uint16_t* wd = (const uint16_t*)d_in[6];
  char* ws = (char*)d_ws;
  (void)hipFuncSetAttribute((const void*)k1_qkv, hipFuncAttributeMaxDynamicSharedMemorySize, K1_LDS);
  (void)hipFuncSetAttribute((const void*)k2_attn, hipFuncAttributeMaxDynamicSharedMemorySize, K2_LDS);
  (void)hipFuncSetAttribute((const void*)k4_ffn_up, hipFuncAttributeMaxDynamicSharedMemorySize, K4_LDS);
  k1_qkv<<<256, 512, K1_LDS, stream>>>(x, wqkv, ws);
  k2_attn<<<256, 512, K2_LDS, stream>>>(wo, wg, wu, wd, ws);
  k3_oproj<<<252, 256, 65536, stream>>>(x, ws);
  k4_ffn_up<<<256, 256, K4_LDS, stream>>>(ws);
  k5_ffn_down<<<224, 256, 65536, stream>>>(ws, (uint16_t*)d_out);
}
```

```cpp
#include <hip/hip_runtime.h>
#include <hip/hip_bf16.h>
#include <stdint.h>

typedef short bf16x8 __attribute__((ext_vector_type(8)));
typedef short s16x4 __attribute__((ext_vector_type(4)));
typedef float f32x16 __attribute__((ext_vector_type(16)));
typedef float f32x4 __attribute__((ext_vector_type(4)));
typedef float f32x2 __attribute__((ext_vector_type(2)));
typedef unsigned u32x4 __attribute__((ext_vector_type(4)));
typedef unsigned u32x2 __attribute__((ext_vector_type(2)));
typedef __bf16 b16x2 __attribute__((ext_vector_type(2)));
#define DI __device__ __forceinline__
#define MFMA(a, b, c) __builtin_amdgcn_mfma_f32_32x32x16_bf16((a), (b), (c), 0, 0, 0)
#define LDS_AS __attribute__((address_space(3)))
#define WAIT_VM(n) asm volatile("s_waitcnt vmcnt(" #n ")" ::: "memory")
#define WAIT_LGKM0() asm volatile("s_waitcnt lgkmcnt(0)" ::: "memory")
#define BAR() __builtin_amdgcn_s_barrier()
#define SB() __builtin_amdgcn_sched_barrier(0)

constexpr int S_ = 2048, D_ = 448, H_ = 16, F_ = 1792;
constexpr float LN_EPS = 1e-5f;
constexpr float QSCALE = 0.18898223650461362f * 1.4426950408889634f;

constexpr size_t WS_QF = 0;
constexpr size_t WS_KVF = WS_QF + (size_t)16 * 64 * 2048;
constexpr size_t WS_OF = WS_KVF + (size_t)16 * 64 * 4096;
constexpr size_t WS_HF = WS_OF + (size_t)64 * 28 * 1024;
constexpr size_t WS_AF = WS_HF + (size_t)64 * 28 * 1024;
constexpr size_t WS_WOF = WS_AF + (size_t)64 * 112 * 1024;
constexpr size_t WS_WGF = WS_WOF + (size_t)14 * 28 * 1024;
constexpr size_t WS_WDF = WS_WGF + (size_t)112 * 28 * 1024;
constexpr size_t WS_ST = WS_WDF + (size_t)14 * 112 * 1024;
constexpr size_t WS_CS = WS_ST + (size_t)14 * 2048 * 8;
constexpr size_t WS_END = WS_CS + 112 * 1024;
#define TS_DECL
#define TS(i)
#define TS_OUT(k)
#define CY0()
#define CY1()

DI unsigned pk2(float a, float b) {
  b16x2 v = {(__bf16)a, (__bf16)b};
  return __builtin_bit_cast(unsigned, v);
}
DI float bf_lo(unsigned u) { return __uint_as_float(u << 16); }
DI float bf_hi(unsigned u) { return __uint_as_float(u & 0xffff0000u); }
DI float rbf(float a) { return (float)(__bf16)a; }
DI float dot2(unsigned a, unsigned b, float c) {
  return __builtin_amdgcn_fdot2_f32_bf16(__builtin_bit_cast(b16x2, a), __builtin_bit_cast(b16x2, b), c, false);
}
DI float xhalf_add(float v) {
  auto r = __builtin_amdgcn_permlane32_swap(__float_as_uint(v), __float_as_uint(v), false, false);
  return __uint_as_float(r[0]) + __uint_as_float(r[1]);
}
DI float xhalf_max(float v) {
  auto r = __builtin_amdgcn_permlane32_swap(__float_as_uint(v), __float_as_uint(v), false, false);
  return fmaxf(__uint_as_float(r[0]), __uint_as_float(r[1]));
}
DI bf16x8 pack8(const f32x16& a, int s) {
  u32x4 w = {pk2(a[8 * s], a[8 * s + 1]), pk2(a[8 * s + 2], a[8 * s + 3]), pk2(a[8 * s + 4], a[8 * s + 5]),
             pk2(a[8 * s + 6], a[8 * s + 7])};
  return __builtin_bit_cast(bf16x8, w);
}
DI void glds16(const void* g, void* l) {
  __builtin_amdgcn_global_load_lds((const __attribute__((address_space(1))) unsigned*)g, (LDS_AS unsigned*)l, 16, 0, 0);
}
template <int OFF>
DI void glds16o(const char* ubase, unsigned voff, void* l) {
  __builtin_amdgcn_global_load_lds((const __attribute__((address_space(1))) unsigned*)(ubase + voff), (LDS_AS unsigned*)l, 16, OFF, 0);
}
DI s16x4 tr_read(const char* p) { return __builtin_amdgcn_ds_read_tr16_b64_v4i16((LDS_AS s16x4*)p); }
DI u32x2 neg_split3(float m) {
  float n = -m;
  float a = rbf(n), r1 = n - a, b = rbf(r1), r2 = r1 - b;
  return u32x2{pk2(a, b), pk2(r2, 0.f)};
}
constexpr unsigned ONES2 = 0x3f803f80u;
template <int N> struct IC { static constexpr int value = N; };
template <int I, int N, class F>
DI void static_for(F&& f) { if constexpr (I < N) { f(IC<I>{}); static_for<I + 1, N>(f); } }

constexpr int K1_WIMG = 448 * 64;
constexpr int K1_XOFF = 3 * K1_WIMG;
constexpr int K1_TB = K1_XOFF + 4 * 4 * 4096;
constexpr int K1_DUMMY = K1_TB + 3584;
constexpr int K1_LDS = K1_DUMMY + 256;

DI void split3(float v, float& a, float& b, float& c) { a = rbf(v); const float r1 = v - a; b = rbf(r1); c = r1 - b; }
DI bf16x8 corr_c(float v, int hh) {
  float a, b, c; split3(v, a, b, c);
  u32x4 o = {pk2(a, a), pk2(a, b), pk2(b, b), pk2(c, c)};
  if (hh) o = u32x4{pk2(c, 0.f), 0u, 0u, 0u};
  return __builtin_bit_cast(bf16x8, o);
}
DI bf16x8 corr_m(float v, int hh) {
  float a, b, c; split3(v, a, b, c);
  u32x4 o = {pk2(a, b), pk2(c, a), pk2(b, c), pk2(a, b)};
  if (hh) o = u32x4{pk2(c, 0.f), 0u, 0u, 0u};
  return __builtin_bit_cast(bf16x8, o);
}
DI void st16(void* p, u32x4 v) { asm volatile("global_store_dwordx4 %0, %1, off sc1\n\ts_nop 1" :: "v"(p), "v"(v) : "memory"); }
DI void touch(const void* p, void* dummy) {
  __builtin_amdgcn_global_load_lds((const __attribute__((address_space(1))) unsigned*)p, (LDS_AS unsigned*)dummy, 4, 0, 0);
}
DI void st8(void* p, u32x2 v) { asm volatile("global_store_dwordx2 %0, %1, off sc1" :: "v"(p), "v"(v) : "memory"); }

static __device__ void code_end();
DI const void* code_line(unsigned line) {
  const uintptr_t a = (uintptr_t)__builtin_amdgcn_s_getpc() + (uintptr_t)line * 128, hi = (uintptr_t)&code_end;
  return (const void*)(a < hi ? a : hi);
}

__global__ __launch_bounds__(512, 2) void k1_qkv(const uint16_t* __restrict__ x, const uint16_t* __restrict__ wqkv,
                                                 char* __restrict__ ws) {
  extern __shared__ __attribute__((aligned(16))) char sm[];
  const int tid = threadIdx.x, lane = tid & 63, w = __builtin_amdgcn_readfirstlane(tid >> 6), wq = w & 3;
  const int b = blockIdx.x, xcd = b & 7, idx = b >> 3;
  const int tt = (xcd >> 1) * 4 + (idx & 3), h = (xcd & 1) * 8 + (idx >> 2);
  const int r = lane & 31, hh = lane >> 5;
  TS_DECL; TS(0);
  const char* wsrc = (const char*)wqkv + (size_t)(16 * wq + (lane >> 2)) * 2688 + ((56 * h) & ~15) + (lane & 3) * 16;
  const char* xsrc0 = (const char*)x + (size_t)(tt * 128 + wq * 32 + (lane >> 3)) * 896 + (((lane & 7) ^ (lane >> 4)) * 16);
  const char* xsrc1 = (const char*)x + (size_t)(tt * 128 + wq * 32 + 8 + (lane >> 3)) * 896 + (((lane & 7) ^ (4 + (lane >> 4))) * 16);
  char* xl = sm + K1_XOFF + wq * 16384;
#define K1_LDX(kc)                                                                                          \
  _Pragma("unroll") for (int t = 0; t < 2; ++t) {                                                           \
    glds16(xsrc0 + (size_t)t * 16 * 896 + (kc) * 128, xl + ((kc) & 3) * 4096 + t * 2048);                   \
    glds16(xsrc1 + (size_t)t * 16 * 896 + (kc) * 128, xl + ((kc) & 3) * 4096 + t * 2048 + 1024);            \
  }
#define K1_LDW(kc)                                                                                          \
  _Pragma("unroll") for (int s = 0; s < 3; ++s)                                                             \
      glds16(wsrc + (size_t)(kc) * 64 * 2688 + s * 896, sm + s * K1_WIMG + ((kc) * 64 + 16 * wq) * 64);
  const size_t tile = (size_t)h * 64 + tt * 4 + wq;
  if (w >= 4) { K1_LDX(0) touch(code_line(wq * 28 + (lane & 31)), sm + K1_DUMMY); K1_LDX(1) K1_LDX(2) K1_LDX(3) }
  else {
    K1_LDW(0)
    const int t2 = tid < 168 ? tid : tid - 88, t3 = tid >= 144 ? tid - 144 : tid % 112;
    touch((const char*)wqkv + (size_t)(idx * 14 + t2 / 12) * 2688 + ((t2 % 12) >> 2) * 896 + ((xcd & 1) * 3 + (t2 & 3)) * 128, sm + K1_DUMMY);
    touch((const char*)x + (size_t)((xcd >> 1) * 512 + idx * 16 + t3 / 7) * 896 + (t3 % 7) * 128, sm + K1_DUMMY);
    touch(lane == 63 ? (const char*)code_line(96 + b * 4 + wq) : ws + (hh ? WS_QF + tile * 2048 : WS_KVF + tile * 4096), sm + K1_DUMMY);
    K1_LDW(1) K1_LDW(2) K1_LDW(3)
  }
  TS(1);
  const unsigned lbase = (unsigned)(uintptr_t)(LDS_AS char*)sm;
  const int xsw = (r >> 1) & 7;
  unsigned xa[4], wa[4];
#pragma unroll
  for (int s = 0; s < 4; ++s) {
    const int sr = (s + wq) & 3;
    xa[s] = lbase + K1_XOFF + wq * 16384 + r * 128 + (((2 * sr + hh) ^ xsw) * 16);
    wa[s] = lbase + (w >= 4 ? 2 * K1_WIMG : 0) + (16 * sr + 8 * hh + ((lane & 15) >> 2)) * 64 + 8 * (h & 1) + 32 * ((lane >> 4) & 1) + 8 * (lane & 3);
    if (w < 4 && (lane & 19) == 19) wa[s] = lbase + ((h & 1) ? 0 : 56);
  }
  const unsigned onea = lbase + ((h & 1) ? 0 : 56) + lane * 256;
#define K1_ONES(kc) if (w == 0 && lane < 2) asm volatile("ds_write_b64 %0, %1 offset:%2" :: "v"(onea), "v"(u32x2{0x3f80u, 0u}), "i"((kc) * 4096) : "memory");
  float* tb = (float*)(sm + K1_TB);
  bf16x8 xf[3];
  s16x4 wl[3][4];
  if (w < 4) {
    f32x16 aq = {}, ak = {};
    float csq = 0.f, csk = 0.f, sq0 = 0.f, sq1 = 0.f;
#define K1_RDA(n)                                                                                            \
  {                                                                                                          \
    constexpr int kc_ = (n) >> 2, s_ = (n) & 3, b_ = (n) % 3;                                                \
    asm volatile("ds_read_b128 %0, %1 offset:%2" : "=v"(xf[b_]) : "v"(xa[s_]), "i"((kc_ & 3) * 4096));       \
    asm volatile("ds_read_b64_tr_b16 %0, %1 offset:%2" : "=v"(wl[b_][0]) : "v"(wa[s_]), "i"(kc_ * 4096));    \
    asm volatile("ds_read_b64_tr_b16 %0, %1 offset:%2" : "=v"(wl[b_][1]) : "v"(wa[s_]), "i"(kc_ * 4096 + 256)); \
    asm volatile("ds_read_b64_tr_b16 %0, %1 offset:%2" : "=v"(wl[b_][2]) : "v"(wa[s_]), "i"(K1_WIMG + kc_ * 4096)); \
    asm volatile("ds_read_b64_tr_b16 %0, %1 offset:%2" : "=v"(wl[b_][3]) : "v"(wa[s_]), "i"(K1_WIMG + kc_ * 4096 + 256)); \
  }
#define K1_WTA(c) asm volatile("s_waitcnt lgkmcnt(" #c ")" : "+v"(xf[bb]), "+v"(wl[bb][0]), "+v"(wl[bb][1]), "+v"(wl[bb][2]), "+v"(wl[bb][3]));
    WAIT_VM(12);
    K1_ONES(0)
    WAIT_LGKM0();
    BAR();
    asm volatile("" ::: "memory");
    TS(2);
    K1_RDA(0) K1_RDA(1)
    K1_LDW(4)
    auto step = [&](auto nn) __attribute__((always_inline)) {
      constexpr int n = decltype(nn)::value, kc = n >> 2, s = n & 3, bb = n % 3;
      if (n + 2 < 28) {
        if (s == 2) {
          if (kc < 3) WAIT_VM(9); else if (kc == 3) WAIT_VM(6); else if (kc == 4) WAIT_VM(3); else WAIT_VM(0);
          K1_ONES(kc + 1)
          WAIT_LGKM0(); BAR(); asm volatile("" ::: "memory");
        }
        K1_RDA(n + 2)
        if (s == 2) { if (kc == 0) { K1_LDW(5) } else if (kc == 1) { K1_LDW(6) } }
        K1_WTA(10)
      } else if (n == 26) { K1_WTA(5) } else { K1_WTA(0) }
      SB();
      const bf16x8 w0 = __builtin_shufflevector(wl[bb][0], wl[bb][1], 0, 1, 2, 3, 4, 5, 6, 7);
      const bf16x8 w1 = __builtin_shufflevector(wl[bb][2], wl[bb][3], 0, 1, 2, 3, 4, 5, 6, 7);
      aq = MFMA(w0, xf[bb], aq);
      ak = MFMA(w1, xf[bb], ak);
      const u32x4 xu = __builtin_bit_cast(u32x4, xf[bb]);
#pragma unroll
      for (int j = 0; j < 2; ++j) { const float lo = bf_lo(xu[j]), hi = bf_hi(xu[j]); sq0 = fmaf(lo, lo, sq0); sq1 = fmaf(hi, hi, sq1); }
      asm volatile("" : "+v"(sq0)); asm volatile("" : "+v"(sq1));
      if (s == 0) {
        const u32x4 u0 = __builtin_bit_cast(u32x4, w0), u1 = __builtin_bit_cast(u32x4, w1);
#pragma unroll
        for (int j = 0; j < 4; ++j) { csq = dot2(u0[j], ONES2, csq); csk = dot2(u1[j], ONES2, csk); }
      }
      if (n == 25) {
        csq = xhalf_add(csq); csk = xhalf_add(csk);
        if (hh == 0) { tb[wq * 32 + r] = csq; tb[128 + wq * 32 + r] = csk; }
      }
      SB();
      if (n == 3) TS(3);
      if (n == 15) TS(4);
    };
#define K1_S4(k) step(IC<4 * k>{}); step(IC<4 * k + 1>{}); step(IC<4 * k + 2>{}); step(IC<4 * k + 3>{});
    K1_S4(0) K1_S4(1) K1_S4(2) K1_S4(3) K1_S4(4) K1_S4(5) K1_S4(6)
    TS(5);
    {
      const float sqa = xhalf_add(sq0 + sq1);
      tb[(hh ? 384 : 512) + wq * 32 + r] = hh ? aq[12] : sqa;
    }
    WAIT_LGKM0(); BAR(); asm volatile("" ::: "memory");
    const float cq = (tb[r] + tb[32 + r]) + (tb[64 + r] + tb[96 + r]);
    const float ck = (tb[128 + r] + tb[160 + r]) + (tb[192 + r] + tb[224 + r]);
    const float mean = tb[384 + wq * 32 + r] * (1.f / 448.f);
    const float rstd = __builtin_amdgcn_rsqf(fmaxf((tb[512 + wq * 32 + r] + tb[640 + wq * 32 + r]) * (1.f / 448.f) - mean * mean, 0.f) + LN_EPS);
    const bf16x8 mf = corr_m(-mean, hh);
    aq = MFMA(corr_c(cq, hh), mf, aq);
    ak = MFMA(corr_c(ck, hh), mf, ak);
    const float rq = rstd * QSCALE;
    float sii = 0.f;
    u32x4 qo[2], ko[2];
#pragma unroll
    for (int g = 0; g < 4; ++g) {
      unsigned q0 = pk2(aq[4 * g] * rq, aq[4 * g + 1] * rq), q1 = pk2(aq[4 * g + 2] * rq, aq[4 * g + 3] * rq);
      unsigned k0 = pk2(ak[4 * g] * rstd, ak[4 * g + 1] * rstd), k1 = pk2(ak[4 * g + 2] * rstd, ak[4 * g + 3] * rstd);
      if (g == 3 && hh == 1) { q0 = 0; q1 = 0; k0 = ONES2; k1 = 0x00003f80u; }
      sii = dot2(q0, k0, sii); sii = dot2(q1, k1, sii);
      qo[g >> 1][2 * (g & 1)] = q0; qo[g >> 1][2 * (g & 1) + 1] = q1;
      ko[g >> 1][2 * (g & 1)] = k0; ko[g >> 1][2 * (g & 1) + 1] = k1;
    }
    char* qd = ws + WS_QF + tile * 2048 + lane * 16;
    char* kd = ws + WS_KVF + tile * 4096 + lane * 16;
    st16(kd, ko[0]); st16(kd + 1024, ko[1]); st16(qd, qo[0]);
    sii = xhalf_add(sii);
    if (hh == 1) { u32x2 ms = neg_split3(sii); qo[1][2] = ms[0]; qo[1][3] = ms[1]; }
    st16(qd + 1024, qo[1]);
  } else {
    f32x16 av = {};
    float csv = 0.f, sq0 = 0.f, sq1 = 0.f;
#define K1_RDB(n)                                                                                            \
  {                                                                                                          \
    constexpr int kc_ = (n) >> 2, s_ = (n) & 3, b_ = (n) % 3;                                                \
    asm volatile("ds_read_b128 %0, %1 offset:%2" : "=v"(xf[b_]) : "v"(xa[s_]), "i"((kc_ & 3) * 4096));       \
    asm volatile("ds_read_b64_tr_b16 %0, %1 offset:%2" : "=v"(wl[b_][0]) : "v"(wa[s_]), "i"(kc_ * 4096));    \
    asm volatile("ds_read_b64_tr_b16 %0, %1 offset:%2" : "=v"(wl[b_][1]) : "v"(wa[s_]), "i"(kc_ * 4096 + 256)); \
  }
#define K1_WTB(c) asm volatile("s_waitcnt lgkmcnt(" #c ")" : "+v"(xf[bb]), "+v"(wl[bb][0]), "+v"(wl[bb][1]));
    WAIT_VM(13);
    BAR();
    asm volatile("" ::: "memory");
    TS(2);
    K1_RDB(0) K1_RDB(1)
    auto step = [&](auto nn) __attribute__((always_inline)) {
      constexpr int n = decltype(nn)::value, kc = n >> 2, s = n & 3, bb = n % 3;
      if (n + 2 < 28) {
        if (s == 2) {
          WAIT_LGKM0();
          if (kc < 4) WAIT_VM(8); else if (kc == 4) WAIT_VM(4); else WAIT_VM(0);
          BAR();
          asm volatile("" ::: "memory");
        }
        K1_RDB(n + 2)
        if (s == 2) { if (kc == 0) { K1_LDX(4) } else if (kc == 1) { K1_LDX(5) } else if (kc == 2) { K1_LDX(6) } }
        K1_WTB(6)
      } else if (n == 26) { K1_WTB(3) } else { K1_WTB(0) }
      SB();
      const bf16x8 w2 = __builtin_shufflevector(wl[bb][0], wl[bb][1], 0, 1, 2, 3, 4, 5, 6, 7);
      av = MFMA(xf[bb], w2, av);
      const u32x4 xu = __builtin_bit_cast(u32x4, xf[bb]);
#pragma unroll
      for (int j = 2; j < 4; ++j) { const float lo = bf_lo(xu[j]), hi = bf_hi(xu[j]); sq0 = fmaf(lo, lo, sq0); sq1 = fmaf(hi, hi, sq1); }
      asm volatile("" : "+v"(sq0)); asm volatile("" : "+v"(sq1));
      if (s == 0) {
        const u32x4 u2 = __builtin_bit_cast(u32x4, w2);
#pragma unroll
        for (int j = 0; j < 4; ++j) csv = dot2(u2[j], ONES2, csv);
      }
      SB();
      if (n == 3) TS(3);
      if (n == 15) TS(4);
    };
    K1_S4(0) K1_S4(1) K1_S4(2) K1_S4(3) K1_S4(4) K1_S4(5) K1_S4(6)
    TS(5);
    {
      const float sqb = xhalf_add(sq0 + sq1);
      csv = xhalf_add(csv);
      tb[(hh ? 256 : 640) + wq * 32 + r] = hh ? csv : sqb;
    }
    WAIT_LGKM0(); BAR(); asm volatile("" ::: "memory");
    const float cv = (tb[256 + r] + tb[288 + r]) + (tb[320 + r] + tb[352 + r]);
    const float mean = tb[384 + wq * 32 + r] * (1.f / 448.f);
    const float rstd = __builtin_amdgcn_rsqf(fmaxf((tb[512 + wq * 32 + r] + tb[640 + wq * 32 + r]) * (1.f / 448.f) - mean * mean, 0.f) + LN_EPS);
    tb[768 + wq * 32 + r] = rstd;
    f32x4 rs[4];
#pragma unroll
    for (int g = 0; g < 4; ++g) rs[g] = *(const f32x4*)(tb + 768 + wq * 32 + 8 * g + 4 * hh);
    av = MFMA(corr_m(-mean, hh), corr_c(cv, hh), av);
    u32x4 vo[2];
#pragma unroll
    for (int g = 0; g < 4; ++g) {
      unsigned v0 = pk2(av[4 * g] * rs[g][0], av[4 * g + 1] * rs[g][1]), v1 = pk2(av[4 * g + 2] * rs[g][2], av[4 * g + 3] * rs[g][3]);
      if (r >= 28) v0 = v1 = (r == 28) ? ONES2 : 0u;
      vo[g >> 1][2 * (g & 1)] = v0; vo[g >> 1][2 * (g & 1) + 1] = v1;
    }
    char* kd = ws + WS_KVF + tile * 4096 + lane * 16;
    st16(kd + 2048, vo[0]); st16(kd + 3072, vo[1]);
  }
  TS_OUT(0);
}

constexpr int K2_QL = 8 * 16384;
constexpr int K2_LS = K2_QL + 8192;
constexpr int K2_DUMMY = K2_LS + 4096;
constexpr int K2_LDS = K2_DUMMY + 256;

__global__ __launch_bounds__(512, 2) void k2_attn(const uint16_t* __restrict__ wo, const uint16_t* __restrict__ wg,
                                                  const uint16_t* __restrict__ wu, const uint16_t* __restrict__ wd,
                                                  char* __restrict__ ws) {
  extern __shared__ __attribute__((aligned(16))) char sm[];
  const int tid = threadIdx.x, lane = tid & 63, w = __builtin_amdgcn_readfirstlane(tid >> 6);
  const int b = blockIdx.x, xcd = b & 7, idx = b >> 3;
  const int h = 2 * xcd + (idx & 1), qt = idx >> 1;
  const int r = lane & 31, hh = lane >> 5;
  TS_DECL; TS(0);
  const unsigned l16 = lane * 16;
  const unsigned lbase = (unsigned)(uintptr_t)(LDS_AS char*)sm;
  glds16o<0>(ws + WS_QF + ((size_t)h * 64 + qt * 4) * 2048 + w * 1024, l16, sm + K2_QL + w * 1024);
  const char* kvsrc = ws + WS_KVF + ((size_t)h * 64 + w * 8) * 4096;
  bf16x8 kv[4][4];
#define K2_LDKV(t)                                                                                           \
  _Pragma("unroll") for (int c = 0; c < 4; ++c)                                                              \
      asm volatile("global_load_dwordx4 %0, %1, %2 offset:%3" : "=v"(kv[(t) & 3][c]) : "v"(l16), "s"(kvsrc + (t) * 4096), "i"(c * 1024) : "memory");
  K2_LDKV(0)
  touch(code_line(w * 16 + (lane & 15)), sm + K2_DUMMY);
  K2_LDKV(1) K2_LDKV(2)
  SB();
  char* wdst[3];
  const uint16_t* wsrc[3];
  {
    const int gw = b * 8 + w;
#pragma unroll
    for (int it = 0; it < 3; ++it) {
      int f = gw + it * 2048;
      if (f >= 5096) f = gw;
      if (f < 3136) {
        const int cl = f & 3, t = f >> 2, ks = t % 28, c = (t / 28) * 4 + cl;
        wsrc[it] = ((lane & 2) ? wu : wg) + (size_t)(16 * ks + (lane >> 2)) * 1792 + 16 * c + 8 * (lane & 1);
        wdst[it] = ws + WS_WGF + ((size_t)c * 28 + ks) * 1024;
      } else if (f < 3136 + 1568) {
        const int g = f - 3136, nl = g & 1, t = g >> 1, ks = t % 112, nb = (t / 112) * 2 + nl;
        wsrc[it] = wd + (size_t)(16 * ks + (lane >> 2)) * 448 + 32 * nb + 8 * (lane & 3);
        wdst[it] = ws + WS_WDF + ((size_t)nb * 112 + ks) * 1024;
      } else {
        const int g = f - 4704, nl = g & 1, t = g >> 1, ks = t % 28, nb = (t / 28) * 2 + nl;
        wsrc[it] = wo + (size_t)(16 * ks + (lane >> 2)) * 448 + 32 * nb + 8 * (lane & 3);
        wdst[it] = ws + WS_WOF + ((size_t)nb * 28 + ks) * 1024;
      }
      asm volatile("" : "+v"(wsrc[it]));
    }
  }
  TS(1);
  WAIT_VM(13);
  BAR();
  asm volatile("" ::: "memory");
  TS(2); CY0();
  bf16x8 q[4][2];
  const unsigned qa = lbase + K2_QL + l16;
#pragma unroll
  for (int i = 0; i < 4; ++i) {
    asm volatile("ds_read_b128 %0, %1 offset:%2" : "=v"(q[i][0]) : "v"(qa), "i"(i * 2048));
    asm volatile("ds_read_b128 %0, %1 offset:%2" : "=v"(q[i][1]) : "v"(qa), "i"(i * 2048 + 1024));
  }
  K2_LDKV(3)
  asm volatile("s_waitcnt lgkmcnt(0)" : "+v"(q[0][0]), "+v"(q[0][1]), "+v"(q[1][0]), "+v"(q[1][1]), "+v"(q[2][0]), "+v"(q[2][1]), "+v"(q[3][0]), "+v"(q[3][1]));
  float* ls = (float*)(sm + K2_LS);
  auto put = [&](const f32x16& o, int i) __attribute__((always_inline)) {
#pragma unroll
    for (int g = 0; g < 4; ++g)
      *(f32x4*)(sm + w * 16384 + (i * 4 + g) * 1024 + l16) = f32x4{o[4 * g], o[4 * g + 1], o[4 * g + 2], o[4 * g + 3]};
    if (hh == 1) ls[w * 128 + i * 32 + r] = o[12];
  };
  f32x16 o[4], sc[2];
  bf16x8 p1;
  asm volatile("s_waitcnt vmcnt(15)" : "+v"(kv[0][0]), "+v"(kv[0][1]));
  sc[0] = MFMA(kv[0][0], q[0][0], (f32x16{}));
  sc[0] = MFMA(kv[0][1], q[0][1], sc[0]);
  auto tile = [&](auto nn) __attribute__((always_inline)) {
    constexpr int n = decltype(nn)::value, m = n + 1;
    constexpr int t = n < 20 ? n >> 2 : 5 + (n - 20) % 3, i = n < 20 ? n & 3 : (n - 20) / 3;
    constexpr int tn = m < 20 ? m >> 2 : 5 + (m - 20) % 3, in = m < 20 ? m & 3 : ((m - 20) / 3) & 3;
    f32x16& s = sc[n & 1];
    f32x16& sn = sc[m & 1];
    f32x16& oo = o[i];
    bf16x8(&kc)[4] = kv[t & 3];
    bf16x8(&kn)[4] = kv[tn & 3];
#define K2_WTKV(c) asm volatile("s_waitcnt vmcnt(" #c ")" : "+v"(kn[0]), "+v"(kn[1]), "+v"(kn[2]), "+v"(kn[3]));
    if (n == 0) asm volatile("s_waitcnt vmcnt(13)" : "+v"(kv[0][2]), "+v"(kv[0][3]));
    if (n == 3 || n == 19) { K2_WTKV(8) }
    if (n == 7 || n == 11 || n == 15) { K2_WTKV(11) }
    if (n == 20) { K2_WTKV(4) }
    if (n == 21) { K2_WTKV(0) }
    if (n == 0) __builtin_amdgcn_s_setprio(3);
    if (n == 10) __builtin_amdgcn_s_setprio(2);
    if (n == 20) __builtin_amdgcn_s_setprio(1);
    if (n == 27) __builtin_amdgcn_s_setprio(0);
    constexpr int l = n - 1;
    constexpr int tl = l < 20 ? l >> 2 : 5 + (l - 20) % 3, il = l < 20 ? l & 3 : (l - 20) / 3;
    SB();
    if (n > 0) o[il & 3] = MFMA(kv[tl & 3][3], p1, o[il & 3]);
    SB();
    if (n == 4) { K2_LDKV(4) }
    if (n == 8) { K2_LDKV(5) }
    if (n == 12) { K2_LDKV(6) }
    if (n == 16) { K2_LDKV(7) }
#pragma unroll
    for (int e = 0; e < 4; ++e) s[e] = __builtin_amdgcn_exp2f(s[e]);
    SB();
    if (m < 32) sn = MFMA(kn[0], q[in][0], (f32x16{}));
    SB();
    if (n == 4) {
#pragma unroll
      for (int it = 0; it < 3; ++it) glds16(wsrc[it], sm + w * 16384 + 12288 + it * 1024);
    }
#pragma unroll
    for (int e = 4; e < 8; ++e) s[e] = __builtin_amdgcn_exp2f(s[e]);
    const bf16x8 p0 = pack8(s, 0);
    SB();
    if (m < 32) sn = MFMA(kn[1], q[in][1], sn);
    SB();
#pragma unroll
    for (int e = 8; e < 12; ++e) s[e] = __builtin_amdgcn_exp2f(s[e]);
    SB();
    if (t == 0) oo = MFMA(kc[2], p0, (f32x16{})); else oo = MFMA(kc[2], p0, oo);
    SB();
#pragma unroll
    for (int e = 12; e < 16; ++e) s[e] = __builtin_amdgcn_exp2f(s[e]);
    p1 = pack8(s, 1);
    SB();
    if (n == 31) oo = MFMA(kc[3], p1, oo);
    SB();
    if (n == 23) put(o[0], 0);
    if (n == 26) put(o[1], 1);
    if (n == 29) put(o[2], 2);
    if (n == 31) put(o[3], 3);
    if (n == 21) {
      const unsigned wa = lbase + w * 16384 + 12288 + (4 * hh + ((lane & 15) >> 2)) * 64 + 32 * ((lane >> 4) & 1) + 8 * (lane & 3);
      s16x4 wl[3][2];
#pragma unroll
      for (int it = 0; it < 3; ++it) {
        asm volatile("ds_read_b64_tr_b16 %0, %1 offset:%2" : "=v"(wl[it][0]) : "v"(wa), "i"(it * 1024) : "memory");
        asm volatile("ds_read_b64_tr_b16 %0, %1 offset:%2" : "=v"(wl[it][1]) : "v"(wa), "i"(it * 1024 + 512) : "memory");
      }
      asm volatile("s_waitcnt lgkmcnt(0)" : "+v"(wl[0][0]), "+v"(wl[0][1]), "+v"(wl[1][0]), "+v"(wl[1][1]), "+v"(wl[2][0]), "+v"(wl[2][1]) :: "memory");
#pragma unroll
      for (int it = 0; it < 3; ++it)
        if (it < 2 || b * 8 + w + 4096 < 5096)
          st16(wdst[it] + l16, __builtin_bit_cast(u32x4, __builtin_shufflevector(wl[it][0], wl[it][1], 0, 1, 2, 3, 4, 5, 6, 7)));
    }
    SB();
    if (n == 1) TS(3);
    if (n == 15) TS(4);
  };
#define K2_T4(k) tile(IC<4 * k>{}); tile(IC<4 * k + 1>{}); tile(IC<4 * k + 2>{}); tile(IC<4 * k + 3>{});
  K2_T4(0) K2_T4(1) K2_T4(2) K2_T4(3) K2_T4(4) K2_T4(5) K2_T4(6) K2_T4(7)
  TS(5);
  const int oi = w >> 1, og = 2 * (w & 1);
  f32x4 res[2];
  bool bad;
  auto gather = [&]() __attribute__((always_inline)) {
    __syncthreads();
    float l = 0.f;
    f32x2 la = {};
    res[0] = f32x4{}; res[1] = f32x4{};
#pragma unroll
    for (int ww = 0; ww < 8; ++ww) {
      res[0] += *(const f32x4*)(sm + ww * 16384 + (oi * 4 + og) * 1024 + l16);
      res[1] += *(const f32x4*)(sm + ww * 16384 + (oi * 4 + og + 1) * 1024 + l16);
      l += ls[ww * 128 + oi * 32 + r];
      la += *(const f32x2*)(ls + ww * 128 + 2 * lane);
    }
    const float rl = 1.f / l;
    res[0] *= rl; res[1] *= rl;
    bad = __any(!(fmaxf(la[0], la[1]) < 1.0e9f) || !(la[0] == la[0]) || !(la[1] == la[1]));
  };
  gather();
  CY1();
  if (bad) {
    __syncthreads();
    u32x4 qq[4][2];
#pragma unroll
    for (int i = 0; i < 4; ++i) {
      qq[i][0] = *(const u32x4*)(sm + K2_QL + i * 2048 + l16);
      qq[i][1] = *(const u32x4*)(sm + K2_QL + i * 2048 + 1024 + l16);
      if (hh == 1) { qq[i][1][2] = 0; qq[i][1][3] = 0; }
    }
    float m[4] = {-INFINITY, -INFINITY, -INFINITY, -INFINITY};
#pragma unroll 1
    for (int t = 0; t < 8; ++t) {
      const bf16x8 k0 = *(const bf16x8*)(kvsrc + t * 4096 + l16), k1 = *(const bf16x8*)(kvsrc + t * 4096 + 1024 + l16);
#pragma unroll
      for (int i = 0; i < 4; ++i) {
        f32x16 s = {};
        s = MFMA(k0, __builtin_bit_cast(bf16x8, qq[i][0]), s);
        s = MFMA(k1, __builtin_bit_cast(bf16x8, qq[i][1]), s);
#pragma unroll
        for (int e = 0; e < 16; ++e) m[i] = fmaxf(m[i], s[e]);
      }
    }
#pragma unroll
    for (int i = 0; i < 4; ++i) { m[i] = xhalf_max(m[i]); if (hh == 0) ls[w * 128 + i * 32 + r] = m[i]; }
    __syncthreads();
#pragma unroll
    for (int i = 0; i < 4; ++i) {
      float mm = -INFINITY;
#pragma unroll
      for (int ww = 0; ww < 8; ++ww) mm = fmaxf(mm, ls[ww * 128 + i * 32 + r]);
      const u32x2 ms = neg_split3(mm);
      if (hh == 1) { qq[i][1][2] = ms[0]; qq[i][1][3] = ms[1]; }
    }
    __syncthreads();
    f32x16 of[4] = {};
#pragma unroll 1
    for (int t = 0; t < 8; ++t) {
      const bf16x8 k0 = *(const bf16x8*)(kvsrc + t * 4096 + l16), k1 = *(const bf16x8*)(kvsrc + t * 4096 + 1024 + l16);
      const bf16x8 v0 = *(const bf16x8*)(kvsrc + t * 4096 + 2048 + l16), v1 = *(const bf16x8*)(kvsrc + t * 4096 + 3072 + l16);
#pragma unroll
      for (int i = 0; i < 4; ++i) {
        f32x16 s = {};
        s = MFMA(k0, __builtin_bit_cast(bf16x8, qq[i][0]), s);
        s = MFMA(k1, __builtin_bit_cast(bf16x8, qq[i][1]), s);
#pragma unroll
        for (int e = 0; e < 16; ++e) s[e] = __builtin_amdgcn_exp2f(s[e]);
        of[i] = MFMA(v0, pack8(s, 0), of[i]);
        of[i] = MFMA(v1, pack8(s, 1), of[i]);
      }
    }
#pragma unroll
    for (int i = 0; i < 4; ++i) put(of[i], i);
    gather();
  }
#pragma unroll
  for (int c = 0; c < 2; ++c) {
    const int gl = 2 * (og + c) + hh;
    const int gi = 7 * h + gl;
    if (gl < 7) {
      char* od = ws + WS_OF + ((size_t)(qt * 4 + oi) * 28 + (gi >> 2)) * 1024 + ((gi & 1) * 32 + r) * 16 + ((gi >> 1) & 1) * 8;
      *(u32x2*)od = u32x2{pk2(res[c][0], res[c][1]), pk2(res[c][2], res[c][3])};
    }
  }
  TS_OUT(1);
}

template <int T>
DI void red_put(const f32x16& a, char* sm, int w, int lane) {
#pragma unroll
  for (int g = 0; g < 4; ++g)
    *(f32x4*)(sm + ((w * 4 + T) * 4 + g) * 1024 + lane * 16) = f32x4{a[4 * g], a[4 * g + 1], a[4 * g + 2], a[4 * g + 3]};
}
DI f32x16 reduce4(f32x16 (&acc)[2][2], char* sm, int w, int lane) {
  f32x16 o;
  if (w != 0) red_put<0>(acc[0][0], sm, w, lane); else o = acc[0][0];
  if (w != 1) red_put<1>(acc[0][1], sm, w, lane); else o = acc[0][1];
  if (w != 2) red_put<2>(acc[1][0], sm, w, lane); else o = acc[1][0];
  if (w != 3) red_put<3>(acc[1][1], sm, w, lane); else o = acc[1][1];
  __syncthreads();
  f32x4 p[3][4];
#pragma unroll
  for (int i = 0; i < 3; ++i) {
    const int ww = (w + 1 + i) & 3;
#pragma unroll
    for (int g = 0; g < 4; ++g) p[i][g] = *(const f32x4*)(sm + ((ww * 4 + w) * 4 + g) * 1024 + lane * 16);
  }
#pragma unroll
  for (int g = 0; g < 4; ++g)
#pragma unroll
    for (int j = 0; j < 4; ++j) o[4 * g + j] = (o[4 * g + j] + p[0][g][j]) + (p[1][g][j] + p[2][g][j]);
  return o;
}

__global__ __launch_bounds__(256, 1) void k3_oproj(const uint16_t* __restrict__ x, char* __restrict__ ws) {
  extern __shared__ __attribute__((aligned(16))) char sm[];
  const int tid = threadIdx.x, lane = tid & 63, w = __builtin_amdgcn_readfirstlane(tid >> 6);
  const int b = blockIdx.x, r = lane & 31, hh = lane >> 5;
  TS_DECL; TS(0);
  if (b >= 224) {
    const int c = (b - 224) * 4 + w;
    const char* src = ws + WS_WGF + (size_t)c * 28 * 1024 + lane * 16;
    u32x4 v[28];
#pragma unroll
    for (int i = 0; i < 28; ++i) v[i] = *(const u32x4*)(src + i * 1024);
    SB();
    float s0 = 0.f, s1 = 0.f;
#pragma unroll
    for (int i = 0; i < 28; ++i) { s0 = dot2(v[i][0], ONES2, s0); s1 = dot2(v[i][1], ONES2, s1); s0 = dot2(v[i][2], ONES2, s0); s1 = dot2(v[i][3], ONES2, s1); }
    const float s = xhalf_add(s0 + s1);
    *(bf16x8*)(ws + WS_CS + (size_t)c * 1024 + lane * 16) = corr_c(s, hh);
    TS_OUT(2);
    return;
  }
  const int xcd = b & 7, idx = b >> 3, t64 = 4 * xcd + (idx & 3), nb = idx >> 2;
  const char* osrc = ws + WS_OF + ((size_t)(2 * t64) * 28 + 7 * w) * 1024 + lane * 16;
  const char* wsrc = ws + WS_WOF + ((size_t)(2 * nb) * 28 + 7 * w) * 1024 + lane * 16;
  bf16x8 of[7][2], wf[7][2];
#pragma unroll
  for (int s = 0; s < 7; ++s) {
    of[s][0] = *(const bf16x8*)(osrc + s * 1024); wf[s][0] = *(const bf16x8*)(wsrc + s * 1024);
    of[s][1] = *(const bf16x8*)(osrc + (28 + s) * 1024); wf[s][1] = *(const bf16x8*)(wsrc + (28 + s) * 1024);
  }
  const int tt32 = 2 * t64 + (w & 1), n32 = 2 * nb + (w >> 1);
  const uint16_t* xs = x + (size_t)(tt32 * 32 + r) * 448 + n32 * 32 + 4 * hh;
  u32x2 xr[4];
#pragma unroll
  for (int g = 0; g < 4; ++g) xr[g] = *(const u32x2*)(xs + 8 * g);
  SB();
  TS(1);
  f32x16 acc[2][2] = {};
#pragma unroll
  for (int s = 0; s < 7; ++s) {
#pragma unroll
    for (int c = 0; c < 2; ++c)
#pragma unroll
      for (int a = 0; a < 2; ++a) acc[c][a] = MFMA(wf[s][c], of[s][a], acc[c][a]);
    SB();
  }
  TS(2);
  f32x16 o = reduce4(acc, sm, w, lane);
  TS(3);
  float s1 = 0.f, s2 = 0.f;
  u32x4 ho[2];
#pragma unroll
  for (int g = 0; g < 4; ++g) {
    const float h0 = bf_lo(xr[g][0]) + rbf(o[4 * g]), h1 = bf_hi(xr[g][0]) + rbf(o[4 * g + 1]);
    const float h2 = bf_lo(xr[g][1]) + rbf(o[4 * g + 2]), h3 = bf_hi(xr[g][1]) + rbf(o[4 * g + 3]);
    const unsigned p0 = pk2(h0, h1), p1 = pk2(h2, h3);
    s1 = dot2(p0, ONES2, s1); s1 = dot2(p1, ONES2, s1);
    s2 = dot2(p0, p0, s2); s2 = dot2(p1, p1, s2);
    ho[g >> 1][2 * (g & 1)] = p0; ho[g >> 1][2 * (g & 1) + 1] = p1;
  }
  char* hd = ws + WS_HF + ((size_t)tt32 * 28 + 2 * n32) * 1024 + lane * 16;
  *(u32x4*)hd = ho[0]; *(u32x4*)(hd + 1024) = ho[1];
  s1 = xhalf_add(s1); s2 = xhalf_add(s2);
  if (hh == 0) *(f32x2*)(ws + WS_ST + ((size_t)n32 * 2048 + tt32 * 32 + r) * 8) = f32x2{s1, s2};
  TS_OUT(2);
}

constexpr int K4_CH = 28 * 1024;
constexpr int K4_CF = 5 * K4_CH;
constexpr int K4_LDS = K4_CF + 8192;
constexpr int k4_step(int e, int i) {
  constexpr signed char t[8][9] = {
    {2, 3, 4, 5, 6, 7, 8, 4, -1},
    {2, 3, 5, 6, 7, 8, 9, 5, 10},
    {2, 3, 6, 9, 10, 11, 12, 8, -1},
    {2, 9, 10, 12, 13, 14, 15, 10, 16},
    {11, 12, 13, 14, 15, 16, 17, 13, -1},
    {13, 15, 16, 17, 18, 19, 20, 17, 21},
    {18, 19, 20, 21, 22, 23, 24, 20, -1},
    {18, 20, 21, 22, 23, 24, 25, 22, 26}};
  return t[e][i];
}

__global__ __launch_bounds__(256, 2) void k4_ffn_up(char* __restrict__ ws) {
  extern __shared__ __attribute__((aligned(16))) char sm[];
  const int tid = threadIdx.x, lane = tid & 63, w = __builtin_amdgcn_readfirstlane(tid >> 6);
  const int b = blockIdx.x, xcd = b & 7, idx = b >> 3;
  const int tt = (xcd & 1) * 8 + (idx & 7), cg = (xcd >> 1) * 4 + (idx >> 3);
  const int r = lane & 31, hh = lane >> 5;
  TS_DECL; TS(0);
  const char* wsrc = ws + WS_WGF + (size_t)cg * 7 * K4_CH + w * 7168;
  const char* hsrc = ws + WS_HF + ((size_t)(tt * 4 + w) * 28) * 1024;
  const unsigned l16 = lane * 16;
#define K4_LDP(c, i) glds16o<((i) & 3) * 1024>(wsrc + (c) * K4_CH + ((i) >> 2) * 4096, l16, sm + ((c) % 5) * K4_CH + w * 7168 + ((i) >> 2) * 4096);
#define K4_LDW(c) { K4_LDP(c, 0) K4_LDP(c, 1) K4_LDP(c, 2) K4_LDP(c, 3) K4_LDP(c, 4) K4_LDP(c, 5) K4_LDP(c, 6) }
#define K4_LDH(i) asm volatile("global_load_dwordx4 %0, %1, %2 offset:%3" : "=v"(hf[i]) : "v"(l16), "s"(hsrc + ((i) >> 2) * 4096), "i"(((i) & 3) * 1024) : "memory");
  K4_LDW(0)
  f32x2 st[7];
  bf16x8 hf[28];
  {
    const char* csrc = ws + WS_CS + (size_t)cg * 7168;
    glds16o<0>(csrc + w * 1024, l16, sm + K4_CF + w * 1024);
    glds16o<0>(csrc + (w < 3 ? 4 + w : 6) * 1024, l16, sm + K4_CF + (4 + w) * 1024);
    touch(code_line(w * 24 + (lane & 31)), sm + K4_CF + 7 * 1024);
    const unsigned so = (hh * 7 * 2048 + r) * 8;
    const char* ssrc = ws + WS_ST + (size_t)(tt * 128 + w * 32) * 8;
#pragma unroll
    for (int i = 0; i < 7; ++i)
      asm volatile("global_load_dwordx2 %0, %1, %2" : "=v"(st[i]) : "v"(so), "s"(ssrc + i * 16384) : "memory");
  }
#pragma unroll
  for (int i = 0; i < 12; ++i) K4_LDH(i)
  TS(1);
  WAIT_VM(20);
  BAR();
  asm volatile("" ::: "memory");
  TS(2);
  constexpr int PD = 10;
  bf16x8 wf[PD], cf;
  const unsigned lb = (unsigned)(uintptr_t)(LDS_AS char*)sm + l16;
  const unsigned lb1 = lb + 2 * K4_CH, lb2 = lb + 4 * K4_CH;
#define K4_RD(dst, c, ks)                                                                                    \
  {                                                                                                          \
    constexpr int sl_ = (c) % 5;                                                                             \
    asm volatile("ds_read_b128 %0, %1 offset:%2" : "=v"(dst) : "v"(sl_ < 2 ? lb : sl_ < 4 ? lb1 : lb2), "i"((sl_ & 1) * K4_CH + (ks) * 1024)); \
  }
#pragma unroll
  for (int i = 0; i < PD; ++i) K4_RD(wf[i], 0, i)
  SB();
  char* adst = ws + WS_AF + ((size_t)(tt * 4 + w) * 112 + cg * 7) * 1024 + lane * 16;
  constexpr float GC1 = -2.f * 0.7978845608028654f * 1.4426950408889634f, GC3 = GC1 * 0.044715f;
  asm volatile("s_waitcnt vmcnt(12)" : "+v"(st[0]), "+v"(st[1]), "+v"(st[2]), "+v"(st[3]), "+v"(st[4]), "+v"(st[5]), "+v"(st[6]));
  float k1, k3, ve;
  bf16x8 mf;
  {
    float s1 = ((st[0][0] + st[1][0]) + (st[2][0] + st[3][0])) + ((st[4][0] + st[5][0]) + st[6][0]);
    float s2 = ((st[0][1] + st[1][1]) + (st[2][1] + st[3][1])) + ((st[4][1] + st[5][1]) + st[6][1]);
    s1 = xhalf_add(s1); s2 = xhalf_add(s2);
    const float mean = s1 * (1.f / 448.f);
    ve = fmaxf(s2 * (1.f / 448.f) - mean * mean, 0.f) + LN_EPS;
    const float rstd = __builtin_amdgcn_rsqf(ve);
    k1 = GC1 * rstd; k3 = GC3 * rstd * rstd * rstd;
    mf = corr_m(-mean, hh);
  }
  f32x16 acc[2];
  asm volatile("s_waitcnt lgkmcnt(6)" : "+v"(wf[0]), "+v"(wf[1]), "+v"(wf[2]), "+v"(wf[3]));
  SB();
  static_for<0, 8>([&](auto cc) __attribute__((always_inline)) {
    constexpr int c = decltype(cc)::value, A = c & 1;
    float pr[8], ez[8];
    unsigned ap[4];
    static_for<0, 28>([&](auto kk) __attribute__((always_inline)) {
      constexpr int ks = decltype(kk)::value, g = c * 28 + ks;
      if (c < 7) {
        if (c == 0 && !(ks & 1) && ks < 20) {
          asm volatile("s_waitcnt vmcnt(%2)" : "+v"(hf[ks]), "+v"(hf[(ks + 1) % 28]) : "i"(10 + 2 * (ks < 15 ? ks : 15) - ks));
          SB();
        }
        if (ks == 0) acc[A] = MFMA(wf[g % PD], hf[ks], (f32x16{}));
        else acc[A] = MFMA(wf[g % PD], hf[ks], acc[A]);
        SB();
        if (ks + PD == 28 && c < 3) {
          if (c == 0)
            asm volatile("s_waitcnt vmcnt(7) lgkmcnt(0)" : "+v"(hf[20]), "+v"(hf[21]), "+v"(hf[22]), "+v"(hf[23]), "+v"(hf[24]), "+v"(hf[25]), "+v"(hf[26]), "+v"(hf[27]) :: "memory");
          else if (c == 1) asm volatile("s_waitcnt vmcnt(21) lgkmcnt(0)" ::: "memory");
          else WAIT_VM(1);
          BAR();
          asm volatile("" ::: "memory");
        }
        if (ks + PD < 28) K4_RD(wf[g % PD], c, ks + PD)
        else if (c < 6) K4_RD(wf[g % PD], c + 1, (ks + PD) % 28)
        if (c == 0) {
          if (ks < 8) { K4_LDH((12 + 2 * ks) % 28) K4_LDH((13 + 2 * ks) % 28) }
#define K4_P2(k, c0, i0, c1, i1) if (ks == k) { K4_LDP(c0, i0) K4_LDP(c1, i1) }
          K4_P2(8, 1, 0, 1, 1) K4_P2(9, 1, 2, 1, 3) K4_P2(10, 1, 4, 1, 5) K4_P2(11, 1, 6, 2, 0) K4_P2(12, 2, 1, 2, 2) K4_P2(13, 2, 3, 2, 4) K4_P2(14, 2, 5, 2, 6)
          if (ks == 18) { K4_LDW(3) }
          if (ks == 21) { K4_LDW(4) }
          if (ks == 24) { K4_LDW(5) }
        }
        if (c == 1 && ks == 18) { K4_LDW(6) }
        if (ks == 4) asm volatile("ds_read_b128 %0, %1 offset:%2" : "=v"(cf) : "v"(lb2), "i"(K4_CH + c * 1024));
        if ((ks & 3) == 3 && g < 195) {
          constexpr int n = (g + PD < 195 ? g + PD : 195) - (g + 4) + (ks == 7 ? 1 : 0);
          asm volatile("s_waitcnt lgkmcnt(%4)" : "+v"(wf[(g + 1) % PD]), "+v"(wf[(g + 2) % PD]), "+v"(wf[(g + 3) % PD]), "+v"(wf[(g + 4) % PD]) : "i"(n));
          SB();
        }
      }
      if (c > 0) {
        const f32x16& a = acc[A ^ 1];
        static_for<0, 8>([&](auto ee) __attribute__((always_inline)) {
          constexpr int e = decltype(ee)::value;
#define K4_PIN(v) if (c < 7) asm volatile("" : "+v"(v));
          if (k4_step(e, 0) == ks) { ez[e] = a[e] * a[e]; K4_PIN(ez[e]) }
          if (k4_step(e, 1) == ks) { ez[e] = fmaf(ez[e], k3, k1); K4_PIN(ez[e]) }
          if (k4_step(e, 2) == ks) { ez[e] = a[e] * ez[e]; K4_PIN(ez[e]) }
          if (k4_step(e, 3) == ks) { ez[e] = __builtin_amdgcn_exp2f(ez[e]); K4_PIN(ez[e]) }
          if (k4_step(e, 4) == ks) { ez[e] = fmaf(ez[e], ve, ve); K4_PIN(ez[e]) }
          if (k4_step(e, 5) == ks) { ez[e] = __builtin_amdgcn_rcpf(ez[e]); K4_PIN(ez[e]) }
          if (k4_step(e, 6) == ks) { ez[e] = pr[e] * ez[e]; K4_PIN(ez[e]) }
          if (k4_step(e, 7) == ks) { pr[e] = a[e] * a[8 + e]; K4_PIN(pr[e]) }
          if (k4_step(e, 8) == ks) { ap[e >> 1] = pk2(ez[e & 6], ez[e]); K4_PIN(ap[e >> 1]) }
        });
      }
      if (ks == 27) {
        if (c < 7) { SB(); asm volatile("" : "+v"(cf)); acc[A] = MFMA(cf, mf, acc[A]); SB(); }
        if (c > 0) st16(adst + (c - 1) * 1024, u32x4{ap[0], ap[1], ap[2], ap[3]});
      }
      if (c < 7) SB();
    });
    if (c == 0) TS(3);
    if (c == 2) { TS(4); CY0(); }
    if (c == 6) { TS(5); CY1(); }
  });
  TS_OUT(3);
}

__global__ __launch_bounds__(256, 1) void k5_ffn_down(char* __restrict__ ws, uint16_t* __restrict__ out) {
  extern __shared__ __attribute__((aligned(16))) char sm[];
  const int tid = threadIdx.x, lane = tid & 63, w = __builtin_amdgcn_readfirstlane(tid >> 6);
  const int b = blockIdx.x, r = lane & 31, hh = lane >> 5;
  TS_DECL; TS(0);
  const int xcd = b & 7, idx = b >> 3, t64 = 4 * xcd + (idx & 3), nb = idx >> 2;
  const char* asrc = ws + WS_AF + ((size_t)(2 * t64) * 112 + 28 * w) * 1024 + lane * 16;
  const char* wsrc = ws + WS_WDF + ((size_t)(2 * nb) * 112 + 28 * w) * 1024 + lane * 16;
  constexpr int P = 14;
  bf16x8 af[P][2], wf[P][2];
#pragma unroll
  for (int s = 0; s < P; ++s) {
    af[s][0] = *(const bf16x8*)(asrc + s * 1024); wf[s][0] = *(const bf16x8*)(wsrc + s * 1024);
    af[s][1] = *(const bf16x8*)(asrc + (112 + s) * 1024); wf[s][1] = *(const bf16x8*)(wsrc + (112 + s) * 1024);
  }
  SB();
  TS(1);
  const int tt32 = 2 * t64 + (w & 1), n32 = 2 * nb + (w >> 1);
  const char* hs = ws + WS_HF + ((size_t)tt32 * 28 + 2 * n32) * 1024 + lane * 16;
  u32x4 h0, h1;
  f32x16 acc[2][2] = {};
#pragma unroll
  for (int s = 0; s < 28; ++s) {
#pragma unroll
    for (int c = 0; c < 2; ++c)
#pragma unroll
      for (int a = 0; a < 2; ++a) acc[c][a] = MFMA(wf[s % P][c], af[s % P][a], acc[c][a]);
    SB();
    if (s + P < 28) {
      af[s % P][0] = *(const bf16x8*)(asrc + (s + P) * 1024); wf[s % P][0] = *(const bf16x8*)(wsrc + (s + P) * 1024);
      af[s % P][1] = *(const bf16x8*)(asrc + (112 + s + P) * 1024); wf[s % P][1] = *(const bf16x8*)(wsrc + (112 + s + P) * 1024);
    }
    if (s + P == 28) { h0 = *(const u32x4*)hs; h1 = *(const u32x4*)(hs + 1024); }
    SB();
    if (s == 0) TS(2);
    if (s == 13) TS(3);
  }
  TS(4);
  f32x16 o = reduce4(acc, sm, w, lane);
  TS(5);
  unsigned pq[4][2];
#pragma unroll
  for (int g = 0; g < 4; ++g) {
    const unsigned ha = g < 2 ? h0[2 * (g & 1)] : h1[2 * (g & 1)], hb = g < 2 ? h0[2 * (g & 1) + 1] : h1[2 * (g & 1) + 1];
    pq[g][0] = pk2(bf_lo(ha) + rbf(o[4 * g]), bf_hi(ha) + rbf(o[4 * g + 1]));
    pq[g][1] = pk2(bf_lo(hb) + rbf(o[4 * g + 2]), bf_hi(hb) + rbf(o[4 * g + 3]));
  }
  uint16_t* od = out + (size_t)(tt32 * 32 + r) * 448 + n32 * 32 + 8 * hh;
#pragma unroll
  for (int g = 0; g < 4; g += 2) {
    auto r0 = __builtin_amdgcn_permlane32_swap(pq[g][0], pq[g + 1][0], false, false);
    auto r1 = __builtin_amdgcn_permlane32_swap(pq[g][1], pq[g + 1][1], false, false);
    *(u32x4*)(od + 8 * g) = u32x4{r0[0], r1[0], r0[1], r1[1]};
  }
  TS_OUT(4);
}

static __device__ __attribute__((noinline, used)) void code_end() { asm volatile("s_nop 0"); }

extern "C" void kernel_launch(void* const* d_in, const int* in_sizes, int n_in, void* d_out, int out_size, void* d_ws,
                              size_t ws_size, hipStream_t stream) {
  const uint16_t* x = (const uint16_t*)d_in[0];
  const uint16_t* wqkv = (const uint16_t*)d_in[1];
  const uint16_t* wo = (const uint16_t*)d_in[2];
  const uint16_t* wg = (const uint16_t*)d_in[4];
  const uint16_t* wu = (const uint16_t*)d_in[5];
  const uint16_t* wd = (const uint16_t*)d_in[6];
  char* ws = (char*)d_ws;
  (void)hipFuncSetAttribute((const void*)k1_qkv, hipFuncAttributeMaxDynamicSharedMemorySize, K1_LDS);
  (void)hipFuncSetAttribute((const void*)k2_attn, hipFuncAttributeMaxDynamicSharedMemorySize, K2_LDS);
  (void)hipFuncSetAttribute((const void*)k4_ffn_up, hipFuncAttributeMaxDynamicSharedMemorySize, K4_LDS);
  k1_qkv<<<256, 512, K1_LDS, stream>>>(x, wqkv, ws);
  k2_attn<<<256, 512, K2_LDS, stream>>>(wo, wg, wu, wd, ws);
  k3_oproj<<<252, 256, 65536, stream>>>(x, ws);
  k4_ffn_up<<<256, 256, K4_LDS, stream>>>(ws);
  k5_ffn_down<<<224, 256, 65536, stream>>>(ws, (uint16_t*)d_out);
}
```

```cpp
#include <hip/hip_runtime.h>
#include <hip/hip_bf16.h>
#include <stdint.h>

typedef short bf16x8 __attribute__((ext_vector_type(8)));
typedef short s16x4 __attribute__((ext_vector_type(4)));
typedef float f32x16 __attribute__((ext_vector_type(16)));
typedef float f32x4 __attribute__((ext_vector_type(4)));
typedef float f32x2 __attribute__((ext_vector_type(2)));
typedef unsigned u32x4 __attribute__((ext_vector_type(4)));
typedef unsigned u32x2 __attribute__((ext_vector_type(2)));
typedef __bf16 b16x2 __attribute__((ext_vector_type(2)));
#define DI __device__ __forceinline__
#define MFMA(a, b, c) __builtin_amdgcn_mfma_f32_32x32x16_bf16((a), (b), (c), 0, 0, 0)
#define LDS_AS __attribute__((address_space(3)))
#define WAIT_VM(n) asm volatile("s_waitcnt vmcnt(" #n ")" ::: "memory")
#define WAIT_LGKM0() asm volatile("s_waitcnt lgkmcnt(0)" ::: "memory")
#define BAR() __builtin_amdgcn_s_barrier()
#define SB() __builtin_amdgcn_sched_barrier(0)

constexpr int S_ = 2048, D_ = 448, H_ = 16, F_ = 1792;
constexpr float LN_EPS = 1e-5f;
constexpr float QSCALE = 0.18898223650461362f * 1.4426950408889634f;

constexpr size_t WS_QF = 0;
constexpr size_t WS_KVF = WS_QF + (size_t)16 * 64 * 2048;
constexpr size_t WS_OF = WS_KVF + (size_t)16 * 64 * 4096;
constexpr size_t WS_HF = WS_OF + (size_t)64 * 28 * 1024;
constexpr size_t WS_AF = WS_HF + (size_t)64 * 28 * 1024;
constexpr size_t WS_WOF = WS_AF + (size_t)64 * 112 * 1024;
constexpr size_t WS_WGF = WS_WOF + (size_t)14 * 28 * 1024;
constexpr size_t WS_WDF = WS_WGF + (size_t)112 * 28 * 1024;
constexpr size_t WS_ST = WS_WDF + (size_t)14 * 112 * 1024;
constexpr size_t WS_CS = WS_ST + (size_t)14 * 2048 * 8;
constexpr size_t WS_CNT = WS_CS + 112 * 1024;
constexpr size_t WS_END = WS_CNT + 32 * 128;
#define TS_DECL
#define TS(i)
#define TS_OUT(k)
#define CY0()
#define CY1()

DI unsigned pk2(float a, float b) {
  b16x2 v = {(__bf16)a, (__bf16)b};
  return __builtin_bit_cast(unsigned, v);
}
DI float bf_lo(unsigned u) { return __uint_as_float(u << 16); }
DI float bf_hi(unsigned u) { return __uint_as_float(u & 0xffff0000u); }
DI float rbf(float a) { return (float)(__bf16)a; }
DI float dot2(unsigned a, unsigned b, float c) {
  return __builtin_amdgcn_fdot2_f32_bf16(__builtin_bit_cast(b16x2, a), __builtin_bit_cast(b16x2, b), c, false);
}
DI float xhalf_add(float v) {
  auto r = __builtin_amdgcn_permlane32_swap(__float_as_uint(v), __float_as_uint(v), false, false);
  return __uint_as_float(r[0]) + __uint_as_float(r[1]);
}
DI float xhalf_max(float v) {
  auto r = __builtin_amdgcn_permlane32_swap(__float_as_uint(v), __float_as_uint(v), false, false);
  return fmaxf(__uint_as_float(r[0]), __uint_as_float(r[1]));
}
DI bf16x8 pack8(const f32x16& a, int s) {
  u32x4 w = {pk2(a[8 * s], a[8 * s + 1]), pk2(a[8 * s + 2], a[8 * s + 3]), pk2(a[8 * s + 4], a[8 * s + 5]),
             pk2(a[8 * s + 6], a[8 * s + 7])};
  return __builtin_bit_cast(bf16x8, w);
}
DI void glds16(const void* g, void* l) {
  __builtin_amdgcn_global_load_lds((const __attribute__((address_space(1))) unsigned*)g, (LDS_AS unsigned*)l, 16, 0, 0);
}
template <int OFF>
DI void glds16o(const char* ubase, unsigned voff, void* l) {
  __builtin_amdgcn_global_load_lds((const __attribute__((address_space(1))) unsigned*)(ubase + voff), (LDS_AS unsigned*)l, 16, OFF, 0);
}
template <int N>
DI void glds_run(const char* g, unsigned l16, char* l) {
  glds16o<0>(g, l16, l);
  if (N > 1) glds16o<1024>(g, l16, l);
  if (N > 2) glds16o<2048>(g, l16, l);
  if (N > 3) glds16o<3072>(g, l16, l);
  if constexpr (N > 4) glds_run<N - 4>(g + 4096, l16, l + 4096);
}
DI s16x4 tr_read(const char* p) { return __builtin_amdgcn_ds_read_tr16_b64_v4i16((LDS_AS s16x4*)p); }
DI u32x2 neg_split3(float m) {
  float n = -m;
  float a = rbf(n), r1 = n - a, b = rbf(r1), r2 = r1 - b;
  return u32x2{pk2(a, b), pk2(r2, 0.f)};
}
constexpr unsigned ONES2 = 0x3f803f80u;
template <int N> struct IC { static constexpr int value = N; };
template <int I, int N, class F>
DI void static_for(F&& f) { if constexpr (I < N) { f(IC<I>{}); static_for<I + 1, N>(f); } }

constexpr int K1_WIMG = 448 * 64;
constexpr int K1_XOFF = 3 * K1_WIMG;
constexpr int K1_TB = K1_XOFF + 4 * 4 * 4096;
constexpr int K1_DUMMY = K1_TB + 3584;
constexpr int K1_LDS = K1_DUMMY + 256;

DI void split3(float v, float& a, float& b, float& c) { a = rbf(v); const float r1 = v - a; b = rbf(r1); c = r1 - b; }
DI bf16x8 corr_c(float v, int hh) {
  float a, b, c; split3(v, a, b, c);
  u32x4 o = {pk2(a, a), pk2(a, b), pk2(b, b), pk2(c, c)};
  if (hh) o = u32x4{pk2(c, 0.f), 0u, 0u, 0u};
  return __builtin_bit_cast(bf16x8, o);
}
DI bf16x8 corr_m(float v, int hh) {
  float a, b, c; split3(v, a, b, c);
  u32x4 o = {pk2(a, b), pk2(c, a), pk2(b, c), pk2(a, b)};
  if (hh) o = u32x4{pk2(c, 0.f), 0u, 0u, 0u};
  return __builtin_bit_cast(bf16x8, o);
}
DI void st16(void* p, u32x4 v) { asm volatile("global_store_dwordx4 %0, %1, off sc1\n\ts_nop 1" :: "v"(p), "v"(v) : "memory"); }
DI void touch(const void* p, void* dummy) {
  __builtin_amdgcn_global_load_lds((const __attribute__((address_space(1))) unsigned*)p, (LDS_AS unsigned*)dummy, 4, 0, 0);
}
DI void st8(void* p, u32x2 v) { asm volatile("global_store_dwordx2 %0, %1, off sc1" :: "v"(p), "v"(v) : "memory"); }

static __device__ void code_end();
DI const void* code_line(unsigned line) {
  const uintptr_t a = (uintptr_t)__builtin_amdgcn_s_getpc() + (uintptr_t)line * 128, hi = (uintptr_t)&code_end;
  return (const void*)(a < hi ? a : hi);
}

__global__ __launch_bounds__(512, 2) void k1_qkv(const uint16_t* __restrict__ x, const uint16_t* __restrict__ wqkv,
                                                 char* __restrict__ ws) {
  extern __shared__ __attribute__((aligned(16))) char sm[];
  const int tid = threadIdx.x, lane = tid & 63, w = __builtin_amdgcn_readfirstlane(tid >> 6), wq = w & 3;
  const int b = blockIdx.x, xcd = b & 7, idx = b >> 3;
  const int tt = (xcd >> 1) * 4 + (idx & 3), h = (xcd & 1) * 8 + (idx >> 2);
  const int r = lane & 31, hh = lane >> 5;
  TS_DECL; TS(0);
  const char* wsrc = (const char*)wqkv + (size_t)(16 * wq + (lane >> 2)) * 2688 + ((56 * h) & ~15) + (lane & 3) * 16;
  const char* xsrc0 = (const char*)x + (size_t)(tt * 128 + wq * 32 + (lane >> 3)) * 896 + (((lane & 7) ^ (lane >> 4)) * 16);
  const char* xsrc1 = (const char*)x + (size_t)(tt * 128 + wq * 32 + 8 + (lane >> 3)) * 896 + (((lane & 7) ^ (4 + (lane >> 4))) * 16);
  char* xl = sm + K1_XOFF + wq * 16384;
#define K1_LDX(kc)                                                                                          \
  _Pragma("unroll") for (int t = 0; t < 2; ++t) {                                                           \
    glds16(xsrc0 + (size_t)t * 16 * 896 + (kc) * 128, xl + ((kc) & 3) * 4096 + t * 2048);                   \
    glds16(xsrc1 + (size_t)t * 16 * 896 + (kc) * 128, xl + ((kc) & 3) * 4096 + t * 2048 + 1024);            \
  }
#define K1_LDW(kc)                                                                                          \
  _Pragma("unroll") for (int s = 0; s < 3; ++s)                                                             \
      glds16(wsrc + (size_t)(kc) * 64 * 2688 + s * 896, sm + s * K1_WIMG + ((kc) * 64 + 16 * wq) * 64);
  const size_t tile = (size_t)h * 64 + tt * 4 + wq;
  if (w >= 4) { K1_LDX(0) touch(code_line(wq * 28 + (lane & 31)), sm + K1_DUMMY); K1_LDX(1) K1_LDX(2) K1_LDX(3) }
  else {
    K1_LDW(0)
    const int t2 = tid < 168 ? tid : tid - 88, t3 = tid >= 144 ? tid - 144 : tid % 112;
    touch((const char*)wqkv + (size_t)(idx * 14 + t2 / 12) * 2688 + ((t2 % 12) >> 2) * 896 + ((xcd & 1) * 3 + (t2 & 3)) * 128, sm + K1_DUMMY);
    touch((const char*)x + (size_t)((xcd >> 1) * 512 + idx * 16 + t3 / 7) * 896 + (t3 % 7) * 128, sm + K1_DUMMY);
    touch(lane == 63 ? (const char*)code_line(96 + b * 4 + wq) : ws + (hh ? WS_QF + tile * 2048 : WS_KVF + tile * 4096), sm + K1_DUMMY);
    K1_LDW(1) K1_LDW(2) K1_LDW(3)
  }
  TS(1);
  const unsigned lbase = (unsigned)(uintptr_t)(LDS_AS char*)sm;
  const int xsw = (r >> 1) & 7;
  unsigned xa[4], wa[4];
#pragma unroll
  for (int s = 0; s < 4; ++s) {
    const int sr = (s + wq) & 3;
    xa[s] = lbase + K1_XOFF + wq * 16384 + r * 128 + (((2 * sr + hh) ^ xsw) * 16);
    wa[s] = lbase + (w >= 4 ? 2 * K1_WIMG : 0) + (16 * sr + 8 * hh + ((lane & 15) >> 2)) * 64 + 8 * (h & 1) + 32 * ((lane >> 4) & 1) + 8 * (lane & 3);
    if (w < 4 && (lane & 19) == 19) wa[s] = lbase + ((h & 1) ? 0 : 56);
  }
  const unsigned onea = lbase + ((h & 1) ? 0 : 56) + lane * 256;
#define K1_ONES(kc) if (w == 0 && lane < 2) asm volatile("ds_write_b64 %0, %1 offset:%2" :: "v"(onea), "v"(u32x2{0x3f80u, 0u}), "i"((kc) * 4096) : "memory");
  float* tb = (float*)(sm + K1_TB);
  bf16x8 xf[3];
  s16x4 wl[3][4];
  if (w < 4) {
    f32x16 aq = {}, ak = {};
    float csq = 0.f, csk = 0.f, sq0 = 0.f, sq1 = 0.f;
#define K1_RDA(n)                                                                                            \
  {                                                                                                          \
    constexpr int kc_ = (n) >> 2, s_ = (n) & 3, b_ = (n) % 3;                                                \
    asm volatile("ds_read_b128 %0, %1 offset:%2" : "=v"(xf[b_]) : "v"(xa[s_]), "i"((kc_ & 3) * 4096));       \
    asm volatile("ds_read_b64_tr_b16 %0, %1 offset:%2" : "=v"(wl[b_][0]) : "v"(wa[s_]), "i"(kc_ * 4096));    \
    asm volatile("ds_read_b64_tr_b16 %0, %1 offset:%2" : "=v"(wl[b_][1]) : "v"(wa[s_]), "i"(kc_ * 4096 + 256)); \
    asm volatile("ds_read_b64_tr_b16 %0, %1 offset:%2" : "=v"(wl[b_][2]) : "v"(wa[s_]), "i"(K1_WIMG + kc_ * 4096)); \
    asm volatile("ds_read_b64_tr_b16 %0, %1 offset:%2" : "=v"(wl[b_][3]) : "v"(wa[s_]), "i"(K1_WIMG + kc_ * 4096 + 256)); \
  }
#define K1_WTA(c) asm volatile("s_waitcnt lgkmcnt(" #c ")" : "+v"(xf[bb]), "+v"(wl[bb][0]), "+v"(wl[bb][1]), "+v"(wl[bb][2]), "+v"(wl[bb][3]));
    WAIT_VM(12);
    K1_ONES(0)
    WAIT_LGKM0();
    BAR();
    asm volatile("" ::: "memory");
    TS(2);
    K1_RDA(0) K1_RDA(1)
    K1_LDW(4)
    auto step = [&](auto nn) __attribute__((always_inline)) {
      constexpr int n = decltype(nn)::value, kc = n >> 2, s = n & 3, bb = n % 3;
      if (n + 2 < 28) {
        if (s == 2) {
          if (kc < 3) WAIT_VM(9); else if (kc == 3) WAIT_VM(6); else if (kc == 4) WAIT_VM(3); else WAIT_VM(0);
          K1_ONES(kc + 1)
          WAIT_LGKM0(); BAR(); asm volatile("" ::: "memory");
        }
        K1_RDA(n + 2)
        if (s == 2) { if (kc == 0) { K1_LDW(5) } else if (kc == 1) { K1_LDW(6) } }
        K1_WTA(10)
      } else if (n == 26) { K1_WTA(5) } else { K1_WTA(0) }
      SB();
      const bf16x8 w0 = __builtin_shufflevector(wl[bb][0], wl[bb][1], 0, 1, 2, 3, 4, 5, 6, 7);
      const bf16x8 w1 = __builtin_shufflevector(wl[bb][2], wl[bb][3], 0, 1, 2, 3, 4, 5, 6, 7);
      aq = MFMA(w0, xf[bb], aq);
      ak = MFMA(w1, xf[bb], ak);
      const u32x4 xu = __builtin_bit_cast(u32x4, xf[bb]);
#pragma unroll
      for (int j = 0; j < 2; ++j) { const float lo = bf_lo(xu[j]), hi = bf_hi(xu[j]); sq0 = fmaf(lo, lo, sq0); sq1 = fmaf(hi, hi, sq1); }
      asm volatile("" : "+v"(sq0)); asm volatile("" : "+v"(sq1));
      if (s == 0) {
        const u32x4 u0 = __builtin_bit_cast(u32x4, w0), u1 = __builtin_bit_cast(u32x4, w1);
#pragma unroll
        for (int j = 0; j < 4; ++j) { csq = dot2(u0[j], ONES2, csq); csk = dot2(u1[j], ONES2, csk); }
      }
      if (n == 25) {
        csq = xhalf_add(csq); csk = xhalf_add(csk);
        if (hh == 0) { tb[wq * 32 + r] = csq; tb[128 + wq * 32 + r] = csk; }
      }
      SB();
      if (n == 3) TS(3);
      if (n == 15) TS(4);
    };
#define K1_S4(k) step(IC<4 * k>{}); step(IC<4 * k + 1>{}); step(IC<4 * k + 2>{}); step(IC<4 * k + 3>{});
    K1_S4(0) K1_S4(1) K1_S4(2) K1_S4(3) K1_S4(4) K1_S4(5) K1_S4(6)
    TS(5);
    {
      const float sqa = xhalf_add(sq0 + sq1);
      tb[(hh ? 384 : 512) + wq * 32 + r] = hh ? aq[12] : sqa;
    }
    WAIT_LGKM0(); BAR(); asm volatile("" ::: "memory");
    const float cq = (tb[r] + tb[32 + r]) + (tb[64 + r] + tb[96 + r]);
    const float ck = (tb[128 + r] + tb[160 + r]) + (tb[192 + r] + tb[224 + r]);
    const float mean = tb[384 + wq * 32 + r] * (1.f / 448.f);
    const float rstd = __builtin_amdgcn_rsqf(fmaxf((tb[512 + wq * 32 + r] + tb[640 + wq * 32 + r]) * (1.f / 448.f) - mean * mean, 0.f) + LN_EPS);
    const bf16x8 mf = corr_m(-mean, hh);
    aq = MFMA(corr_c(cq, hh), mf, aq);
    ak = MFMA(corr_c(ck, hh), mf, ak);
    const float rq = rstd * QSCALE;
    float sii = 0.f;
    u32x4 qo[2], ko[2];
#pragma unroll
    for (int g = 0; g < 4; ++g) {
      unsigned q0 = pk2(aq[4 * g] * rq, aq[4 * g + 1] * rq), q1 = pk2(aq[4 * g + 2] * rq, aq[4 * g + 3] * rq);
      unsigned k0 = pk2(ak[4 * g] * rstd, ak[4 * g + 1] * rstd), k1 = pk2(ak[4 * g + 2] * rstd, ak[4 * g + 3] * rstd);
      if (g == 3 && hh == 1) { q0 = 0; q1 = 0; k0 = ONES2; k1 = 0x00003f80u; }
      sii = dot2(q0, k0, sii); sii = dot2(q1, k1, sii);
      qo[g >> 1][2 * (g & 1)] = q0; qo[g >> 1][2 * (g & 1) + 1] = q1;
      ko[g >> 1][2 * (g & 1)] = k0; ko[g >> 1][2 * (g & 1) + 1] = k1;
    }
    char* qd = ws + WS_QF + tile * 2048 + lane * 16;
    char* kd = ws + WS_KVF + tile * 4096 + lane * 16;
    st16(kd, ko[0]); st16(kd + 1024, ko[1]); st16(qd, qo[0]);
    sii = xhalf_add(sii);
    if (hh == 1) { u32x2 ms = neg_split3(sii); qo[1][2] = ms[0]; qo[1][3] = ms[1]; }
    st16(qd + 1024, qo[1]);
  } else {
    f32x16 av = {};
    float csv = 0.f, sq0 = 0.f, sq1 = 0.f;
#define K1_RDB(n)                                                                                            \
  {                                                                                                          \
    constexpr int kc_ = (n) >> 2, s_ = (n) & 3, b_ = (n) % 3;                                                \
    asm volatile("ds_read_b128 %0, %1 offset:%2" : "=v"(xf[b_]) : "v"(xa[s_]), "i"((kc_ & 3) * 4096));       \
    asm volatile("ds_read_b64_tr_b16 %0, %1 offset:%2" : "=v"(wl[b_][0]) : "v"(wa[s_]), "i"(kc_ * 4096));    \
    asm volatile("ds_read_b64_tr_b16 %0, %1 offset:%2" : "=v"(wl[b_][1]) : "v"(wa[s_]), "i"(kc_ * 4096 + 256)); \
  }
#define K1_WTB(c) asm volatile("s_waitcnt lgkmcnt(" #c ")" : "+v"(xf[bb]), "+v"(wl[bb][0]), "+v"(wl[bb][1]));
    WAIT_VM(13);
    BAR();
    asm volatile("" ::: "memory");
    TS(2);
    K1_RDB(0) K1_RDB(1)
    auto step = [&](auto nn) __attribute__((always_inline)) {
      constexpr int n = decltype(nn)::value, kc = n >> 2, s = n & 3, bb = n % 3;
      if (n + 2 < 28) {
        if (s == 2) {
          WAIT_LGKM0();
          if (kc < 4) WAIT_VM(8); else if (kc == 4) WAIT_VM(4); else WAIT_VM(0);
          BAR();
          asm volatile("" ::: "memory");
        }
        K1_RDB(n + 2)
        if (s == 2) { if (kc == 0) { K1_LDX(4) } else if (kc == 1) { K1_LDX(5) } else if (kc == 2) { K1_LDX(6) } }
        K1_WTB(6)
      } else if (n == 26) { K1_WTB(3) } else { K1_WTB(0) }
      SB();
      const bf16x8 w2 = __builtin_shufflevector(wl[bb][0], wl[bb][1], 0, 1, 2, 3, 4, 5, 6, 7);
      av = MFMA(xf[bb], w2, av);
      const u32x4 xu = __builtin_bit_cast(u32x4, xf[bb]);
#pragma unroll
      for (int j = 2; j < 4; ++j) { const float lo = bf_lo(xu[j]), hi = bf_hi(xu[j]); sq0 = fmaf(lo, lo, sq0); sq1 = fmaf(hi, hi, sq1); }
      asm volatile("" : "+v"(sq0)); asm volatile("" : "+v"(sq1));
      if (s == 0) {
        const u32x4 u2 = __builtin_bit_cast(u32x4, w2);
#pragma unroll
        for (int j = 0; j < 4; ++j) csv = dot2(u2[j], ONES2, csv);
      }
      SB();
      if (n == 3) TS(3);
      if (n == 15) TS(4);
    };
    K1_S4(0) K1_S4(1) K1_S4(2) K1_S4(3) K1_S4(4) K1_S4(5) K1_S4(6)
    TS(5);
    {
      const float sqb = xhalf_add(sq0 + sq1);
      csv = xhalf_add(csv);
      tb[(hh ? 256 : 640) + wq * 32 + r] = hh ? csv : sqb;
    }
    WAIT_LGKM0(); BAR(); asm volatile("" ::: "memory");
    const float cv = (tb[256 + r] + tb[288 + r]) + (tb[320 + r] + tb[352 + r]);
    const float mean = tb[384 + wq * 32 + r] * (1.f / 448.f);
    const float rstd = __builtin_amdgcn_rsqf(fmaxf((tb[512 + wq * 32 + r] + tb[640 + wq * 32 + r]) * (1.f / 448.f) - mean * mean, 0.f) + LN_EPS);
    tb[768 + wq * 32 + r] = rstd;
    f32x4 rs[4];
#pragma unroll
    for (int g = 0; g < 4; ++g) rs[g] = *(const f32x4*)(tb + 768 + wq * 32 + 8 * g + 4 * hh);
    av = MFMA(corr_m(-mean, hh), corr_c(cv, hh), av);
    u32x4 vo[2];
#pragma unroll
    for (int g = 0; g < 4; ++g) {
      unsigned v0 = pk2(av[4 * g] * rs[g][0], av[4 * g + 1] * rs[g][1]), v1 = pk2(av[4 * g + 2] * rs[g][2], av[4 * g + 3] * rs[g][3]);
      if (r >= 28) v0 = v1 = (r == 28) ? ONES2 : 0u;
      vo[g >> 1][2 * (g & 1)] = v0; vo[g >> 1][2 * (g & 1) + 1] = v1;
    }
    char* kd = ws + WS_KVF + tile * 4096 + lane * 16;
    st16(kd + 2048, vo[0]); st16(kd + 3072, vo[1]);
  }
  TS_OUT(0);
}

constexpr int K2_QL = 8 * 16384;
constexpr int K2_LS = K2_QL + 8192;
constexpr int K2_DUMMY = K2_LS + 4096;
constexpr int K2_LDS = K2_DUMMY + 256;

__global__ __launch_bounds__(512, 2) void k2_attn(const uint16_t* __restrict__ wo, const uint16_t* __restrict__ wg,
                                                  const uint16_t* __restrict__ wu, const uint16_t* __restrict__ wd,
                                                  char* __restrict__ ws) {
  extern __shared__ __attribute__((aligned(16))) char sm[];
  const int tid = threadIdx.x, lane = tid & 63, w = __builtin_amdgcn_readfirstlane(tid >> 6);
  const int b = blockIdx.x, xcd = b & 7, idx = b >> 3;
  const int h = 2 * xcd + (idx & 1), qt = idx >> 1;
  const int r = lane & 31, hh = lane >> 5;
  TS_DECL; TS(0);
  const unsigned l16 = lane * 16;
  const unsigned lbase = (unsigned)(uintptr_t)(LDS_AS char*)sm;
  glds16o<0>(ws + WS_QF + ((size_t)h * 64 + qt * 4) * 2048 + w * 1024, l16, sm + K2_QL + w * 1024);
  const char* kvsrc = ws + WS_KVF + ((size_t)h * 64 + w * 8) * 4096;
  bf16x8 kv[4][4];
#define K2_LDKV(t)                                                                                           \
  _Pragma("unroll") for (int c = 0; c < 4; ++c)                                                              \
      asm volatile("global_load_dwordx4 %0, %1, %2 offset:%3" : "=v"(kv[(t) & 3][c]) : "v"(l16), "s"(kvsrc + (t) * 4096), "i"(c * 1024) : "memory");
  K2_LDKV(0)
  touch(code_line(w * 16 + (lane & 15)), sm + K2_DUMMY);
  K2_LDKV(1) K2_LDKV(2)
  SB();
  char* wdst[3];
  const uint16_t* wsrc[3];
  {
    const int gw = b * 8 + w;
#pragma unroll
    for (int it = 0; it < 3; ++it) {
      int f = gw + it * 2048;
      if (f >= 5096) f = gw;
      if (f < 3136) {
        const int cl = f & 3, t = f >> 2, ks = t % 28, c = (t / 28) * 4 + cl;
        wsrc[it] = ((lane & 2) ? wu : wg) + (size_t)(16 * ks + (lane >> 2)) * 1792 + 16 * c + 8 * (lane & 1);
        wdst[it] = ws + WS_WGF + ((size_t)c * 28 + ks) * 1024;
      } else if (f < 3136 + 1568) {
        const int g = f - 3136, nl = g & 1, t = g >> 1, ks = t % 112, nb = (t / 112) * 2 + nl;
        wsrc[it] = wd + (size_t)(16 * ks + (lane >> 2)) * 448 + 32 * nb + 8 * (lane & 3);
        wdst[it] = ws + WS_WDF + ((size_t)nb * 112 + ks) * 1024;
      } else {
        const int g = f - 4704, nl = g & 1, t = g >> 1, ks = t % 28, nb = (t / 28) * 2 + nl;
        wsrc[it] = wo + (size_t)(16 * ks + (lane >> 2)) * 448 + 32 * nb + 8 * (lane & 3);
        wdst[it] = ws + WS_WOF + ((size_t)nb * 28 + ks) * 1024;
      }
      asm volatile("" : "+v"(wsrc[it]));
    }
  }
  TS(1);
  WAIT_VM(13);
  BAR();
  asm volatile("" ::: "memory");
  TS(2); CY0();
  bf16x8 q[4][2];
  const unsigned qa = lbase + K2_QL + l16;
#pragma unroll
  for (int i = 0; i < 4; ++i) {
    asm volatile("ds_read_b128 %0, %1 offset:%2" : "=v"(q[i][0]) : "v"(qa), "i"(i * 2048));
    asm volatile("ds_read_b128 %0, %1 offset:%2" : "=v"(q[i][1]) : "v"(qa), "i"(i * 2048 + 1024));
  }
  K2_LDKV(3)
  asm volatile("s_waitcnt lgkmcnt(0)" : "+v"(q[0][0]), "+v"(q[0][1]), "+v"(q[1][0]), "+v"(q[1][1]), "+v"(q[2][0]), "+v"(q[2][1]), "+v"(q[3][0]), "+v"(q[3][1]));
  float* ls = (float*)(sm + K2_LS);
  auto put = [&](const f32x16& o, int i) __attribute__((always_inline)) {
#pragma unroll
    for (int g = 0; g < 4; ++g)
      *(f32x4*)(sm + w * 16384 + (i * 4 + g) * 1024 + l16) = f32x4{o[4 * g], o[4 * g + 1], o[4 * g + 2], o[4 * g + 3]};
    if (hh == 1) ls[w * 128 + i * 32 + r] = o[12];
  };
  f32x16 o[4], sc[2];
  bf16x8 p1;
  asm volatile("s_waitcnt vmcnt(15)" : "+v"(kv[0][0]), "+v"(kv[0][1]));
  sc[0] = MFMA(kv[0][0], q[0][0], (f32x16{}));
  sc[0] = MFMA(kv[0][1], q[0][1], sc[0]);
  auto tile = [&](auto nn) __attribute__((always_inline)) {
    constexpr int n = decltype(nn)::value, m = n + 1;
    constexpr int t = n < 20 ? n >> 2 : 5 + (n - 20) % 3, i = n < 20 ? n & 3 : (n - 20) / 3;
    constexpr int tn = m < 20 ? m >> 2 : 5 + (m - 20) % 3, in = m < 20 ? m & 3 : ((m - 20) / 3) & 3;
    f32x16& s = sc[n & 1];
    f32x16& sn = sc[m & 1];
    f32x16& oo = o[i];
    bf16x8(&kc)[4] = kv[t & 3];
    bf16x8(&kn)[4] = kv[tn & 3];
#define K2_WTKV(c) asm volatile("s_waitcnt vmcnt(" #c ")" : "+v"(kn[0]), "+v"(kn[1]), "+v"(kn[2]), "+v"(kn[3]));
    if (n == 0) asm volatile("s_waitcnt vmcnt(13)" : "+v"(kv[0][2]), "+v"(kv[0][3]));
    if (n == 3 || n == 19) { K2_WTKV(8) }
    if (n == 7 || n == 11 || n == 15) { K2_WTKV(11) }
    if (n == 20) { K2_WTKV(4) }
    if (n == 21) { K2_WTKV(0) }
    if (n == 0) __builtin_amdgcn_s_setprio(3);
    if (n == 10) __builtin_amdgcn_s_setprio(2);
    if (n == 20) __builtin_amdgcn_s_setprio(1);
    if (n == 27) __builtin_amdgcn_s_setprio(0);
    constexpr int l = n - 1;
    constexpr int tl = l < 20 ? l >> 2 : 5 + (l - 20) % 3, il = l < 20 ? l & 3 : (l - 20) / 3;
    SB();
    if (n > 0) o[il & 3] = MFMA(kv[tl & 3][3], p1, o[il & 3]);
    SB();
    if (n == 4) { K2_LDKV(4) }
    if (n == 8) { K2_LDKV(5) }
    if (n == 12) { K2_LDKV(6) }
    if (n == 16) { K2_LDKV(7) }
#pragma unroll
    for (int e = 0; e < 4; ++e) s[e] = __builtin_amdgcn_exp2f(s[e]);
    SB();
    if (m < 32) sn = MFMA(kn[0], q[in][0], (f32x16{}));
    SB();
    if (n == 4) {
#pragma unroll
      for (int it = 0; it < 3; ++it) glds16(wsrc[it], sm + w * 16384 + 12288 + it * 1024);
    }
#pragma unroll
    for (int e = 4; e < 8; ++e) s[e] = __builtin_amdgcn_exp2f(s[e]);
    const bf16x8 p0 = pack8(s, 0);
    SB();
    if (m < 32) sn = MFMA(kn[1], q[in][1], sn);
    SB();
#pragma unroll
    for (int e = 8; e < 12; ++e) s[e] = __builtin_amdgcn_exp2f(s[e]);
    SB();
    if (t == 0) oo = MFMA(kc[2], p0, (f32x16{})); else oo = MFMA(kc[2], p0, oo);
    SB();
#pragma unroll
    for (int e = 12; e < 16; ++e) s[e] = __builtin_amdgcn_exp2f(s[e]);
    p1 = pack8(s, 1);
    SB();
    if (n == 31) oo = MFMA(kc[3], p1, oo);
    SB();
    if (n == 23) put(o[0], 0);
    if (n == 26) put(o[1], 1);
    if (n == 29) put(o[2], 2);
    if (n == 31) put(o[3], 3);
    if (n == 21) {
      const unsigned wa = lbase + w * 16384 + 12288 + (4 * hh + ((lane & 15) >> 2)) * 64 + 32 * ((lane >> 4) & 1) + 8 * (lane & 3);
      s16x4 wl[3][2];
#pragma unroll
      for (int it = 0; it < 3; ++it) {
        asm volatile("ds_read_b64_tr_b16 %0, %1 offset:%2" : "=v"(wl[it][0]) : "v"(wa), "i"(it * 1024) : "memory");
        asm volatile("ds_read_b64_tr_b16 %0, %1 offset:%2" : "=v"(wl[it][1]) : "v"(wa), "i"(it * 1024 + 512) : "memory");
      }
      asm volatile("s_waitcnt lgkmcnt(0)" : "+v"(wl[0][0]), "+v"(wl[0][1]), "+v"(wl[1][0]), "+v"(wl[1][1]), "+v"(wl[2][0]), "+v"(wl[2][1]) :: "memory");
#pragma unroll
      for (int it = 0; it < 3; ++it)
        if (it < 2 || b * 8 + w + 4096 < 5096)
          st16(wdst[it] + l16, __builtin_bit_cast(u32x4, __builtin_shufflevector(wl[it][0], wl[it][1], 0, 1, 2, 3, 4, 5, 6, 7)));
    }
    SB();
    if (n == 1) TS(3);
    if (n == 15) TS(4);
  };
#define K2_T4(k) tile(IC<4 * k>{}); tile(IC<4 * k + 1>{}); tile(IC<4 * k + 2>{}); tile(IC<4 * k + 3>{});
  K2_T4(0) K2_T4(1) K2_T4(2) K2_T4(3) K2_T4(4) K2_T4(5) K2_T4(6) K2_T4(7)
  TS(5);
  const int oi = w >> 1, og = 2 * (w & 1);
  f32x4 res[2];
  bool bad;
  auto gather = [&]() __attribute__((always_inline)) {
    __syncthreads();
    float l = 0.f;
    f32x2 la = {};
    res[0] = f32x4{}; res[1] = f32x4{};
#pragma unroll
    for (int ww = 0; ww < 8; ++ww) {
      res[0] += *(const f32x4*)(sm + ww * 16384 + (oi * 4 + og) * 1024 + l16);
      res[1] += *(const f32x4*)(sm + ww * 16384 + (oi * 4 + og + 1) * 1024 + l16);
      l += ls[ww * 128 + oi * 32 + r];
      la += *(const f32x2*)(ls + ww * 128 + 2 * lane);
    }
    const float rl = 1.f / l;
    res[0] *= rl; res[1] *= rl;
    bad = __any(!(fmaxf(la[0], la[1]) < 1.0e9f) || !(la[0] == la[0]) || !(la[1] == la[1]));
  };
  gather();
  CY1();
  if (bad) {
    __syncthreads();
    u32x4 qq[4][2];
#pragma unroll
    for (int i = 0; i < 4; ++i) {
      qq[i][0] = *(const u32x4*)(sm + K2_QL + i * 2048 + l16);
      qq[i][1] = *(const u32x4*)(sm + K2_QL + i * 2048 + 1024 + l16);
      if (hh == 1) { qq[i][1][2] = 0; qq[i][1][3] = 0; }
    }
    float m[4] = {-INFINITY, -INFINITY, -INFINITY, -INFINITY};
#pragma unroll 1
    for (int t = 0; t < 8; ++t) {
      const bf16x8 k0 = *(const bf16x8*)(kvsrc + t * 4096 + l16), k1 = *(const bf16x8*)(kvsrc + t * 4096 + 1024 + l16);
#pragma unroll
      for (int i = 0; i < 4; ++i) {
        f32x16 s = {};
        s = MFMA(k0, __builtin_bit_cast(bf16x8, qq[i][0]), s);
        s = MFMA(k1, __builtin_bit_cast(bf16x8, qq[i][1]), s);
#pragma unroll
        for (int e = 0; e < 16; ++e) m[i] = fmaxf(m[i], s[e]);
      }
    }
#pragma unroll
    for (int i = 0; i < 4; ++i) { m[i] = xhalf_max(m[i]); if (hh == 0) ls[w * 128 + i * 32 + r] = m[i]; }
    __syncthreads();
#pragma unroll
    for (int i = 0; i < 4; ++i) {
      float mm = -INFINITY;
#pragma unroll
      for (int ww = 0; ww < 8; ++ww) mm = fmaxf(mm, ls[ww * 128 + i * 32 + r]);
      const u32x2 ms = neg_split3(mm);
      if (hh == 1) { qq[i][1][2] = ms[0]; qq[i][1][3] = ms[1]; }
    }
    __syncthreads();
    f32x16 of[4] = {};
#pragma unroll 1
    for (int t = 0; t < 8; ++t) {
      const bf16x8 k0 = *(const bf16x8*)(kvsrc + t * 4096 + l16), k1 = *(const bf16x8*)(kvsrc + t * 4096 + 1024 + l16);
      const bf16x8 v0 = *(const bf16x8*)(kvsrc + t * 4096 + 2048 + l16), v1 = *(const bf16x8*)(kvsrc + t * 4096 + 3072 + l16);
#pragma unroll
      for (int i = 0; i < 4; ++i) {
        f32x16 s = {};
        s = MFMA(k0, __builtin_bit_cast(bf16x8, qq[i][0]), s);
        s = MFMA(k1, __builtin_bit_cast(bf16x8, qq[i][1]), s);
#pragma unroll
        for (int e = 0; e < 16; ++e) s[e] = __builtin_amdgcn_exp2f(s[e]);
        of[i] = MFMA(v0, pack8(s, 0), of[i]);
        of[i] = MFMA(v1, pack8(s, 1), of[i]);
      }
    }
#pragma unroll
    for (int i = 0; i < 4; ++i) put(of[i], i);
    gather();
  }
#pragma unroll
  for (int c = 0; c < 2; ++c) {
    const int gl = 2 * (og + c) + hh;
    const int gi = 7 * h + gl;
    if (gl < 7) {
      char* od = ws + WS_OF + ((size_t)(qt * 4 + oi) * 28 + (gi >> 2)) * 1024 + ((gi & 1) * 32 + r) * 16 + ((gi >> 1) & 1) * 8;
      *(u32x2*)od = u32x2{pk2(res[c][0], res[c][1]), pk2(res[c][2], res[c][3])};
    }
  }
  TS_OUT(1);
}

template <int T>
DI void red_put(const f32x16& a, char* sm, int w, int lane) {
#pragma unroll
  for (int g = 0; g < 4; ++g)
    *(f32x4*)(sm + ((w * 4 + T) * 4 + g) * 1024 + lane * 16) = f32x4{a[4 * g], a[4 * g + 1], a[4 * g + 2], a[4 * g + 3]};
}
DI f32x16 reduce4(f32x16 (&acc)[2][2], char* sm, int w, int lane) {
  f32x16 o;
  if (w != 0) red_put<0>(acc[0][0], sm, w, lane); else o = acc[0][0];
  if (w != 1) red_put<1>(acc[0][1], sm, w, lane); else o = acc[0][1];
  if (w != 2) red_put<2>(acc[1][0], sm, w, lane); else o = acc[1][0];
  if (w != 3) red_put<3>(acc[1][1], sm, w, lane); else o = acc[1][1];
  __syncthreads();
  f32x4 p[3][4];
#pragma unroll
  for (int i = 0; i < 3; ++i) {
    const int ww = (w + 1 + i) & 3;
#pragma unroll
    for (int g = 0; g < 4; ++g) p[i][g] = *(const f32x4*)(sm + ((ww * 4 + w) * 4 + g) * 1024 + lane * 16);
  }
#pragma unroll
  for (int g = 0; g < 4; ++g)
#pragma unroll
    for (int j = 0; j < 4; ++j) o[4 * g + j] = (o[4 * g + j] + p[0][g][j]) + (p[1][g][j] + p[2][g][j]);
  return o;
}

DI void out_store(const f32x16& o, const u32x4& h0, const u32x4& h1, uint16_t* od) {
  unsigned pq[4][2];
#pragma unroll
  for (int g = 0; g < 4; ++g) {
    const unsigned ha = g < 2 ? h0[2 * (g & 1)] : h1[2 * (g & 1)], hb = g < 2 ? h0[2 * (g & 1) + 1] : h1[2 * (g & 1) + 1];
    pq[g][0] = pk2(bf_lo(ha) + rbf(o[4 * g]), bf_hi(ha) + rbf(o[4 * g + 1]));
    pq[g][1] = pk2(bf_lo(hb) + rbf(o[4 * g + 2]), bf_hi(hb) + rbf(o[4 * g + 3]));
  }
#pragma unroll
  for (int g = 0; g < 4; g += 2) {
    auto r0 = __builtin_amdgcn_permlane32_swap(pq[g][0], pq[g + 1][0], false, false);
    auto r1 = __builtin_amdgcn_permlane32_swap(pq[g][1], pq[g + 1][1], false, false);
    *(u32x4*)(od + 8 * g) = u32x4{r0[0], r1[0], r0[1], r1[1]};
  }
}

__global__ __launch_bounds__(256, 1) void k3_oproj(const uint16_t* __restrict__ x, char* __restrict__ ws) {
  extern __shared__ __attribute__((aligned(16))) char sm[];
  const int tid = threadIdx.x, lane = tid & 63, w = __builtin_amdgcn_readfirstlane(tid >> 6);
  const int b = blockIdx.x, r = lane & 31, hh = lane >> 5;
  TS_DECL; TS(0);
  if (b >= 224) {
    if (b == 224 && tid < 32) *(unsigned*)(ws + WS_CNT + tid * 128) = 0u;
    const int c = (b - 224) * 4 + w;
    const char* src = ws + WS_WGF + (size_t)c * 28 * 1024 + lane * 16;
    u32x4 v[28];
#pragma unroll
    for (int i = 0; i < 28; ++i) v[i] = *(const u32x4*)(src + i * 1024);
    SB();
    float s0 = 0.f, s1 = 0.f;
#pragma unroll
    for (int i = 0; i < 28; ++i) { s0 = dot2(v[i][0], ONES2, s0); s1 = dot2(v[i][1], ONES2, s1); s0 = dot2(v[i][2], ONES2, s0); s1 = dot2(v[i][3], ONES2, s1); }
    const float s = xhalf_add(s0 + s1);
    *(bf16x8*)(ws + WS_CS + (size_t)c * 1024 + lane * 16) = corr_c(s, hh);
    TS_OUT(2);
    return;
  }
  const int xcd = b & 7, idx = b >> 3, t64 = 4 * xcd + (idx & 3), nb = idx >> 2;
  const char* osrc = ws + WS_OF + ((size_t)(2 * t64) * 28 + 7 * w) * 1024 + lane * 16;
  const char* wsrc = ws + WS_WOF + ((size_t)(2 * nb) * 28 + 7 * w) * 1024 + lane * 16;
  bf16x8 of[7][2], wf[7][2];
#pragma unroll
  for (int s = 0; s < 7; ++s) {
    of[s][0] = *(const bf16x8*)(osrc + s * 1024); wf[s][0] = *(const bf16x8*)(wsrc + s * 1024);
    of[s][1] = *(const bf16x8*)(osrc + (28 + s) * 1024); wf[s][1] = *(const bf16x8*)(wsrc + (28 + s) * 1024);
  }
  const int tt32 = 2 * t64 + (w & 1), n32 = 2 * nb + (w >> 1);
  const uint16_t* xs = x + (size_t)(tt32 * 32 + r) * 448 + n32 * 32 + 4 * hh;
  u32x2 xr[4];
#pragma unroll
  for (int g = 0; g < 4; ++g) xr[g] = *(const u32x2*)(xs + 8 * g);
  SB();
  TS(1);
  f32x16 acc[2][2] = {};
#pragma unroll
  for (int s = 0; s < 7; ++s) {
#pragma unroll
    for (int c = 0; c < 2; ++c)
#pragma unroll
      for (int a = 0; a < 2; ++a) acc[c][a] = MFMA(wf[s][c], of[s][a], acc[c][a]);
    SB();
  }
  TS(2);
  f32x16 o = reduce4(acc, sm, w, lane);
  TS(3);
  float s1 = 0.f, s2 = 0.f;
  u32x4 ho[2];
#pragma unroll
  for (int g = 0; g < 4; ++g) {
    const float h0 = bf_lo(xr[g][0]) + rbf(o[4 * g]), h1 = bf_hi(xr[g][0]) + rbf(o[4 * g + 1]);
    const float h2 = bf_lo(xr[g][1]) + rbf(o[4 * g + 2]), h3 = bf_hi(xr[g][1]) + rbf(o[4 * g + 3]);
    const unsigned p0 = pk2(h0, h1), p1 = pk2(h2, h3);
    s1 = dot2(p0, ONES2, s1); s1 = dot2(p1, ONES2, s1);
    s2 = dot2(p0, p0, s2); s2 = dot2(p1, p1, s2);
    ho[g >> 1][2 * (g & 1)] = p0; ho[g >> 1][2 * (g & 1) + 1] = p1;
  }
  char* hd = ws + WS_HF + ((size_t)tt32 * 28 + 2 * n32) * 1024 + lane * 16;
  *(u32x4*)hd = ho[0]; *(u32x4*)(hd + 1024) = ho[1];
  s1 = xhalf_add(s1); s2 = xhalf_add(s2);
  if (hh == 0) *(f32x2*)(ws + WS_ST + ((size_t)n32 * 2048 + tt32 * 32 + r) * 8) = f32x2{s1, s2};
  TS_OUT(2);
}

constexpr int K4_CH = 28 * 1024;
constexpr int K4_CF = 5 * K4_CH;
constexpr int K4_LDS = K4_CF + 8192;
constexpr int KF_LDS = 160 * 1024;
constexpr int k4_step(int e, int i) {
  constexpr signed char t[8][9] = {
    {2, 3, 4, 5, 6, 7, 8, 4, -1},
    {2, 3, 5, 6, 7, 8, 9, 5, 10},
    {2, 3, 6, 9, 10, 11, 12, 8, -1},
    {2, 9, 10, 12, 13, 14, 15, 10, 16},
    {11, 12, 13, 14, 15, 16, 17, 13, -1},
    {13, 15, 16, 17, 18, 19, 20, 17, 21},
    {18, 19, 20, 21, 22, 23, 24, 20, -1},
    {18, 20, 21, 22, 23, 24, 25, 22, 26}};
  return t[e][i];
}

template <int SLEEP>
DI void kf_signal_wait(unsigned* mine, const unsigned* theirs, bool wait, int lane) {
  if (lane == 0) {
    __hip_atomic_fetch_add(mine, 1u, __ATOMIC_RELAXED, __HIP_MEMORY_SCOPE_AGENT);
    if (wait)
      for (int it = 0; it < (1 << 20); ++it) {
        __builtin_amdgcn_s_sleep(SLEEP);
        if (__hip_atomic_load(theirs, __ATOMIC_RELAXED, __HIP_MEMORY_SCOPE_AGENT) >= 16u) break;
      }
  }
}
constexpr int kf_frag(int t) {
  const int p = (t - 16) / 3;
  return t < 16 ? 7 * (t >> 2) + (t & 3) : 7 * (p < 2 ? p + 1 : p == 2 ? 0 : 3) + 4 + (t - 16) % 3;
}
template <bool FUSED>
DI void ffn_body(char* __restrict__ ws, uint16_t* __restrict__ out) {
  extern __shared__ __attribute__((aligned(16))) char sm[];
  const int tid = threadIdx.x, lane = tid & 63, w = __builtin_amdgcn_readfirstlane(tid >> 6);
  const int b = blockIdx.x, xcd = b & 7, idx = b >> 3;
  const int tt = (xcd & 1) * 8 + (idx & 7), cg = (xcd >> 1) * 4 + (idx >> 3);
  const int r = lane & 31, hh = lane >> 5;
  const int t64 = 4 * xcd + (idx & 3), nb = idx >> 2;
  const bool act = idx < 28;
  if (FUSED && w == 4) {
    const char* dl_ = ws + WS_WDF + (size_t)(2 * nb) * 112 * 1024;
    BAR(); BAR(); BAR(); BAR();
    BAR();
    if (act) {
#pragma unroll
      for (int v = 0; v < 4; ++v) {
        glds_run<7>(dl_ + (28 * v + 11) * 1024, lane * 16, sm + 2 * K4_CH + v * 14336);
        glds_run<7>(dl_ + (112 + 28 * v + 11) * 1024, lane * 16, sm + 2 * K4_CH + v * 14336 + 7168);
      }
    }
    BAR();
    if (act) {
#pragma unroll
      for (int v = 0; v < 4; ++v) {
        glds_run<7>(dl_ + (28 * v + 18) * 1024, lane * 16, sm + 4 * K4_CH + v * 7168);
        glds_run<7>(dl_ + (112 + 28 * v + 18) * 1024, lane * 16, sm + v * 7168);
      }
    }
    BAR();
    WAIT_VM(0);
    BAR();
    return;
  }
  if (FUSED && w == 5) {
    unsigned* cnt = (unsigned*)(ws + WS_CNT);
    __builtin_amdgcn_s_setprio(3);
    BAR(); BAR(); BAR(); BAR(); BAR();
    BAR();
    kf_signal_wait<14>(cnt + tt * 32, cnt + (t64 >> 1) * 32, act, lane);
    WAIT_VM(0);
    BAR();
    kf_signal_wait<10>(cnt + (16 + tt) * 32, cnt + (16 + (t64 >> 1)) * 32, act, lane);
    WAIT_VM(0);
    BAR();
    return;
  }
  TS_DECL; TS(0);
  const char* wsrc = ws + WS_WGF + (size_t)cg * 7 * K4_CH + w * 7168;
  const char* hsrc = ws + WS_HF + ((size_t)(tt * 4 + w) * 28) * 1024;
  const unsigned l16 = lane * 16;
#define K4_LDP(c, i) glds16o<((i) & 3) * 1024>(wsrc + (c) * K4_CH + ((i) >> 2) * 4096, l16, sm + ((c) % 5) * K4_CH + w * 7168 + ((i) >> 2) * 4096);
#define K4_LDW(c) { K4_LDP(c, 0) K4_LDP(c, 1) K4_LDP(c, 2) K4_LDP(c, 3) K4_LDP(c, 4) K4_LDP(c, 5) K4_LDP(c, 6) }
#define K4_LDH(i) asm volatile("global_load_dwordx4 %0, %1, %2 offset:%3" : "=v"(hf[i]) : "v"(l16), "s"(hsrc + ((i) >> 2) * 4096), "i"(((i) & 3) * 1024) : "memory");
  K4_LDW(0)
  f32x2 st[7];
  bf16x8 hf[28];
  {
    const char* csrc = ws + WS_CS + (size_t)cg * 7168;
    glds16o<0>(csrc + w * 1024, l16, sm + K4_CF + w * 1024);
    glds16o<0>(csrc + (w < 3 ? 4 + w : 6) * 1024, l16, sm + K4_CF + (4 + w) * 1024);
    touch(code_line(w * 36 + (lane < 36 ? lane : 35)), sm + K4_CF + 7 * 1024);
    const unsigned so = (hh * 7 * 2048 + r) * 8;
    const char* ssrc = ws + WS_ST + (size_t)(tt * 128 + w * 32) * 8;
#pragma unroll
    for (int i = 0; i < 7; ++i)
      asm volatile("global_load_dwordx2 %0, %1, %2" : "=v"(st[i]) : "v"(so), "s"(ssrc + i * 16384) : "memory");
  }
#pragma unroll
  for (int i = 0; i < 12; ++i) K4_LDH(i)
  TS(1);
  WAIT_VM(20);
  BAR();
  asm volatile("" ::: "memory");
  TS(2);
  constexpr int PD = 10;
  bf16x8 wf[PD], cf;
  const unsigned lb = (unsigned)(uintptr_t)(LDS_AS char*)sm + l16;
  const unsigned lb1 = lb + 2 * K4_CH, lb2 = lb + 4 * K4_CH;
#define K4_RD(dst, c, ks)                                                                                    \
  {                                                                                                          \
    constexpr int sl_ = (c) % 5;                                                                             \
    asm volatile("ds_read_b128 %0, %1 offset:%2" : "=v"(dst) : "v"(sl_ < 2 ? lb : sl_ < 4 ? lb1 : lb2), "i"((sl_ & 1) * K4_CH + (ks) * 1024)); \
  }
#pragma unroll
  for (int i = 0; i < PD; ++i) K4_RD(wf[i], 0, i)
  SB();
  char* adst = ws + WS_AF + ((size_t)(tt * 4 + w) * 112 + cg * 7) * 1024 + lane * 16;
  constexpr float GC1 = -2.f * 0.7978845608028654f * 1.4426950408889634f, GC3 = GC1 * 0.044715f;
  asm volatile("s_waitcnt vmcnt(12)" : "+v"(st[0]), "+v"(st[1]), "+v"(st[2]), "+v"(st[3]), "+v"(st[4]), "+v"(st[5]), "+v"(st[6]));
  float k1, k3, ve;
  bf16x8 mf;
  {
    float s1 = ((st[0][0] + st[1][0]) + (st[2][0] + st[3][0])) + ((st[4][0] + st[5][0]) + st[6][0]);
    float s2 = ((st[0][1] + st[1][1]) + (st[2][1] + st[3][1])) + ((st[4][1] + st[5][1]) + st[6][1]);
    s1 = xhalf_add(s1); s2 = xhalf_add(s2);
    const float mean = s1 * (1.f / 448.f);
    ve = fmaxf(s2 * (1.f / 448.f) - mean * mean, 0.f) + LN_EPS;
    const float rstd = __builtin_amdgcn_rsqf(ve);
    k1 = GC1 * rstd; k3 = GC3 * rstd * rstd * rstd;
    mf = corr_m(-mean, hh);
  }
  const char* dsrc = ws + WS_WDF + ((size_t)(2 * (act ? nb : 0)) * 112 + 28 * w) * 1024;
#define KF_LD(dst, base, f) asm volatile("global_load_dwordx4 %0, %1, %2 offset:%3" : "=v"(dst) : "v"(l16), "s"((base) + ((f) >> 2) * 4096), "i"(((f) & 3) * 1024) : "memory");
#define KF_LDA(dst, base, f) asm volatile("global_load_dwordx4 %0, %1, %2 offset:%3 sc1" : "=v"(dst) : "v"(l16), "s"((base) + ((f) >> 2) * 4096), "i"(((f) & 3) * 1024) : "memory");
  bf16x8 dr[8][2];
  f32x16 acc[2];
  asm volatile("s_waitcnt lgkmcnt(6)" : "+v"(wf[0]), "+v"(wf[1]), "+v"(wf[2]), "+v"(wf[3]));
  SB();
  static_for<0, 8>([&](auto cc) __attribute__((always_inline)) {
    constexpr int c = decltype(cc)::value, A = c & 1;
    float pr[8], ez[8];
    unsigned ap[4];
    static_for<0, 28>([&](auto kk) __attribute__((always_inline)) {
      constexpr int ks = decltype(kk)::value, g = c * 28 + ks;
      if (c < 7) {
        if (c == 0 && !(ks & 1) && ks < 20) {
          asm volatile("s_waitcnt vmcnt(%2)" : "+v"(hf[ks]), "+v"(hf[(ks + 1) % 28]) : "i"(10 + 2 * (ks < 15 ? ks : 15) - ks));
          SB();
        }
        if (ks == 0) acc[A] = MFMA(wf[g % PD], hf[ks], (f32x16{}));
        else acc[A] = MFMA(wf[g % PD], hf[ks], acc[A]);
        SB();
        if (ks + PD == 28 && c < 3) {
          if (c == 0)
            asm volatile("s_waitcnt vmcnt(7) lgkmcnt(0)" : "+v"(hf[20]), "+v"(hf[21]), "+v"(hf[22]), "+v"(hf[23]), "+v"(hf[24]), "+v"(hf[25]), "+v"(hf[26]), "+v"(hf[27]) :: "memory");
          else if (c == 1) asm volatile("s_waitcnt vmcnt(21) lgkmcnt(0)" ::: "memory");
          else WAIT_VM(1);
          BAR();
          asm volatile("" ::: "memory");
        }
        if (FUSED && ks == 0 && c == 6) WAIT_VM(1);
        if (FUSED && ks == 0 && (c == 4 || c == 6)) BAR();
        if (ks + PD < 28) K4_RD(wf[g % PD], c, ks + PD)
        else if (c < 6) K4_RD(wf[g % PD], c + 1, (ks + PD) % 28)
        if (c == 0) {
          if (ks < 8) { K4_LDH((12 + 2 * ks) % 28) K4_LDH((13 + 2 * ks) % 28) }
#define K4_P2(k, c0, i0, c1, i1) if (ks == k) { K4_LDP(c0, i0) K4_LDP(c1, i1) }
          K4_P2(8, 1, 0, 1, 1) K4_P2(9, 1, 2, 1, 3) K4_P2(10, 1, 4, 1, 5) K4_P2(11, 1, 6, 2, 0) K4_P2(12, 2, 1, 2, 2) K4_P2(13, 2, 3, 2, 4) K4_P2(14, 2, 5, 2, 6)
          if (ks == 18) { K4_LDW(3) }
          if (ks == 21) { K4_LDW(4) }
          if (ks == 24) { K4_LDW(5) }
        }
        if (c == 1 && ks == 18) { K4_LDW(6) }
        if (ks == 4) asm volatile("ds_read_b128 %0, %1 offset:%2" : "=v"(cf) : "v"(lb2), "i"(K4_CH + c * 1024));
        if ((ks & 3) == 3 && g < 195) {
          constexpr int n = (g + PD < 195 ? g + PD : 195) - (g + 4) + (ks == 7 ? 1 : 0);
          asm volatile("s_waitcnt lgkmcnt(%4)" : "+v"(wf[(g + 1) % PD]), "+v"(wf[(g + 2) % PD]), "+v"(wf[(g + 3) % PD]), "+v"(wf[(g + 4) % PD]) : "i"(n));
          SB();
        }
      }
      if (c > 0) {
        const f32x16& a = acc[A ^ 1];
        static_for<0, 8>([&](auto ee) __attribute__((always_inline)) {
          constexpr int e = decltype(ee)::value;
#define K4_PIN(v) if (c < 7) asm volatile("" : "+v"(v));
          if (k4_step(e, 0) == ks) { ez[e] = a[e] * a[e]; K4_PIN(ez[e]) }
          if (k4_step(e, 1) == ks) { ez[e] = fmaf(ez[e], k3, k1); K4_PIN(ez[e]) }
          if (k4_step(e, 2) == ks) { ez[e] = a[e] * ez[e]; K4_PIN(ez[e]) }
          if (k4_step(e, 3) == ks) { ez[e] = __builtin_amdgcn_exp2f(ez[e]); K4_PIN(ez[e]) }
          if (k4_step(e, 4) == ks) { ez[e] = fmaf(ez[e], ve, ve); K4_PIN(ez[e]) }
          if (k4_step(e, 5) == ks) { ez[e] = __builtin_amdgcn_rcpf(ez[e]); K4_PIN(ez[e]) }
          if (k4_step(e, 6) == ks) { ez[e] = pr[e] * ez[e]; K4_PIN(ez[e]) }
          if (k4_step(e, 7) == ks) { pr[e] = a[e] * a[8 + e]; K4_PIN(pr[e]) }
          if (k4_step(e, 8) == ks) { ap[e >> 1] = pk2(ez[e & 6], ez[e]); K4_PIN(ap[e >> 1]) }
        });
      }
      if (ks == 27) {
        if (c < 7) { SB(); asm volatile("" : "+v"(cf)); acc[A] = MFMA(cf, mf, acc[A]); SB(); }
        if (c > 0) st16(adst + (c - 1) * 1024, u32x4{ap[0], ap[1], ap[2], ap[3]});
      }
      if (c < 7) SB();
    });
    if (c == 0) TS(3);
    if (c == 2) { TS(4); CY0(); }
    if (c == 6) { TS(5); CY1(); }
  });
  TS_OUT(3);
  if (!FUSED) return;
  SB();
  TS(0);
  if (!act) { WAIT_VM(0); BAR(); return; }
#pragma unroll
  for (int i = 0; i < 8; ++i) { KF_LD(dr[i][0], dsrc, kf_frag(i)) KF_LD(dr[i][1], dsrc, 112 + kf_frag(i)) }
  WAIT_VM(16);
  BAR();
  asm volatile("" ::: "memory");
  TS(1);
  __builtin_amdgcn_s_sleep(2);
  const char* asrc = ws + WS_AF + ((size_t)(2 * t64) * 112 + 28 * w) * 1024;
  constexpr int P = 14;
  bf16x8 af[P][2], dl[3][2];
#pragma unroll
  for (int i = 0; i < P; ++i) { KF_LDA(af[i][0], asrc, kf_frag(i)) KF_LDA(af[i][1], asrc, 112 + kf_frag(i)) }
  TS(2);
  const int tt32 = 2 * t64 + (w & 1), n32 = 2 * nb + (w >> 1);
  const char* hs = ws + WS_HF + ((size_t)tt32 * 28 + 2 * n32) * 1024;
  const unsigned ld1 = lb + 2 * K4_CH + w * 14336, ld2 = lb + w * 7168, ld3 = lb + 4 * K4_CH + w * 7168, ld4 = lb + K4_CF + w * 5120;
  u32x4 h0, h1;
  f32x16 oc[2][2];
  static_for<0, 28>([&](auto ss) __attribute__((always_inline)) {
    constexpr int t = decltype(ss)::value;
    constexpr int n = 2 * ((t + P - 1 < 27 ? t + P - 1 : 27) - t) + (t + P > 28 ? 2 : 0) + (t >= 3 && t <= 16 ? 12 : 0);
    asm volatile("s_waitcnt vmcnt(%2)" : "+v"(af[t % P][0]), "+v"(af[t % P][1]) : "i"(n) : "memory");
    if (t < 8) asm volatile("" : "+v"(dr[t % 8][0]), "+v"(dr[t % 8][1]));
    if (t + 2 >= 8 && t + 2 < 28) {
      constexpr int u = t + 2, f = kf_frag(u % 28), x = f >= 25 ? f - 25 : f - 1;
      constexpr bool r1 = f >= 11 && f < 18, r2 = f >= 18 && f < 25;
      asm volatile("ds_read_b128 %0, %1 offset:%2" : "=v"(dl[u % 3][0]) : "v"(r1 ? ld1 : r2 ? ld3 : ld2),
                   "i"(r1 ? (f - 11) * 1024 : r2 ? (f - 18) * 1024 : K4_CH + x * 1024));
      asm volatile("ds_read_b128 %0, %1 offset:%2" : "=v"(dl[u % 3][1]) : "v"(r1 ? ld1 : (r2 || x == 0) ? ld2 : ld4),
                   "i"(r1 ? 7168 + (f - 11) * 1024 : r2 ? (f - 18) * 1024 : x == 0 ? K4_CH + 6144 : (x - 1) * 1024));
    }
    if (t >= 8) asm volatile("s_waitcnt lgkmcnt(%2)" : "+v"(dl[t % 3][0]), "+v"(dl[t % 3][1]) : "i"(t < 26 ? 4 : t == 26 ? 2 : 0));
    SB();
#pragma unroll
    for (int c = 0; c < 2; ++c)
#pragma unroll
      for (int a = 0; a < 2; ++a) {
        const bf16x8 d = t < 8 ? dr[t % 8][c] : dl[t % 3][c];
        if (t == 0) oc[c][a] = MFMA(d, af[t % P][a], (f32x16{})); else oc[c][a] = MFMA(d, af[t % P][a], oc[c][a]);
      }
    SB();
    if (t + P == 16) { BAR(); asm volatile("" ::: "memory"); }
    if (t + P < 28) { KF_LDA(af[t % P][0], asrc, kf_frag((t + P) % 28)) KF_LDA(af[t % P][1], asrc, 112 + kf_frag((t + P) % 28)) }
    if (t == 2) {
      glds_run<3>(dsrc + 25 * 1024, l16, sm + K4_CH + w * 7168);
      glds_run<3>(dsrc + 4 * 1024, l16, sm + K4_CH + w * 7168 + 3072);
      glds_run<1>(dsrc + (112 + 25) * 1024, l16, sm + K4_CH + w * 7168 + 6144);
      glds_run<2>(dsrc + (112 + 26) * 1024, l16, sm + K4_CF + w * 5120);
      glds_run<3>(dsrc + (112 + 4) * 1024, l16, sm + K4_CF + w * 5120 + 2048);
    }
    if (t + P == 28) { KF_LD(h0, hs, 0) KF_LD(h1, hs, 1) }
    SB();
    if (t == 2) TS(3);
  });
  TS(4);
  asm volatile("s_waitcnt vmcnt(0)" : "+v"(h0), "+v"(h1) :: "memory");
  BAR();
  asm volatile("" ::: "memory");
  f32x16 o = reduce4(oc, sm, w, lane);
  TS(5);
  out_store(o, h0, h1, out + (size_t)(tt32 * 32 + r) * 448 + n32 * 32 + 8 * hh);
  TS_OUT(4);
}

__global__ __launch_bounds__(384, 2) void kf_ffn(char* __restrict__ ws, uint16_t* __restrict__ out) { ffn_body<true>(ws, out); }
__global__ __launch_bounds__(256, 2) void k4_ffn_up(char* __restrict__ ws) { ffn_body<false>(ws, nullptr); }

__global__ __launch_bounds__(256, 1) void k5_ffn_down(char* __restrict__ ws, uint16_t* __restrict__ out) {
  extern __shared__ __attribute__((aligned(16))) char sm[];
  const int tid = threadIdx.x, lane = tid & 63, w = __builtin_amdgcn_readfirstlane(tid >> 6);
  const int b = blockIdx.x, r = lane & 31, hh = lane >> 5;
  TS_DECL; TS(0);
  const int xcd = b & 7, idx = b >> 3, t64 = 4 * xcd + (idx & 3), nb = idx >> 2;
  const char* asrc = ws + WS_AF + ((size_t)(2 * t64) * 112 + 28 * w) * 1024 + lane * 16;
  const char* wsrc = ws + WS_WDF + ((size_t)(2 * nb) * 112 + 28 * w) * 1024 + lane * 16;
  constexpr int P = 14;
  bf16x8 af[P][2], wf[P][2];
#pragma unroll
  for (int s = 0; s < P; ++s) {
    af[s][0] = *(const bf16x8*)(asrc + s * 1024); wf[s][0] = *(const bf16x8*)(wsrc + s * 1024);
    af[s][1] = *(const bf16x8*)(asrc + (112 + s) * 1024); wf[s][1] = *(const bf16x8*)(wsrc + (112 + s) * 1024);
  }
  SB();
  TS(1);
  const int tt32 = 2 * t64 + (w & 1), n32 = 2 * nb + (w >> 1);
  const char* hs = ws + WS_HF + ((size_t)tt32 * 28 + 2 * n32) * 1024 + lane * 16;
  u32x4 h0, h1;
  f32x16 acc[2][2] = {};
#pragma unroll
  for (int s = 0; s < 28; ++s) {
#pragma unroll
    for (int c = 0; c < 2; ++c)
#pragma unroll
      for (int a = 0; a < 2; ++a) acc[c][a] = MFMA(wf[s % P][c], af[s % P][a], acc[c][a]);
    SB();
    if (s + P < 28) {
      af[s % P][0] = *(const bf16x8*)(asrc + (s + P) * 1024); wf[s % P][0] = *(const bf16x8*)(wsrc + (s + P) * 1024);
      af[s % P][1] = *(const bf16x8*)(asrc + (112 + s + P) * 1024); wf[s % P][1] = *(const bf16x8*)(wsrc + (112 + s + P) * 1024);
    }
    if (s + P == 28) { h0 = *(const u32x4*)hs; h1 = *(const u32x4*)(hs + 1024); }
    SB();
    if (s == 0) TS(2);
    if (s == 13) TS(3);
  }
  TS(4);
  f32x16 o = reduce4(acc, sm, w, lane);
  TS(5);
  out_store(o, h0, h1, out + (size_t)(tt32 * 32 + r) * 448 + n32 * 32 + 8 * hh);
  TS_OUT(4);
}

static __device__ __attribute__((noinline, used)) void code_end() { asm volatile("s_nop 0"); }

extern "C" void kernel_launch(void* const* d_in, const int* in_sizes, int n_in, void* d_out, int out_size, void* d_ws,
                              size_t ws_size, hipStream_t stream) {
  const uint16_t* x = (const uint16_t*)d_in[0];
  const uint16_t* wqkv = (const uint16_t*)d_in[1];
  const uint16_t* wo = (const uint16_t*)d_in[2];
  const uint16_t* wg = (const uint16_t*)d_in[4];
  const uint16_t* wu = (const uint16_t*)d_in[5];
  const uint16_t* wd = (const uint16_t*)d_in[6];
  char* ws = (char*)d_ws;
  (void)hipFuncSetAttribute((const void*)k1_qkv, hipFuncAttributeMaxDynamicSharedMemorySize, K1_LDS);
  (void)hipFuncSetAttribute((const void*)k2_attn, hipFuncAttributeMaxDynamicSharedMemorySize, K2_LDS);
  (void)hipFuncSetAttribute((const void*)k4_ffn_up, hipFuncAttributeMaxDynamicSharedMemorySize, K4_LDS);
  (void)hipFuncSetAttribute((const void*)kf_ffn, hipFuncAttributeMaxDynamicSharedMemorySize, KF_LDS);
  int dev = 0, cus = 0, occ = 0;
  bool fused = hipGetDevice(&dev) == hipSuccess &&
               hipDeviceGetAttribute(&cus, hipDeviceAttributeMultiprocessorCount, dev) == hipSuccess &&
               hipOccupancyMaxActiveBlocksPerMultiprocessor(&occ, (const void*)kf_ffn, 384, KF_LDS) == hipSuccess &&
               (long)cus * occ >= 256;
  k1_qkv<<<256, 512, K1_LDS, stream>>>(x, wqkv, ws);
  k2_attn<<<256, 512, K2_LDS, stream>>>(wo, wg, wu, wd, ws);
  k3_oproj<<<252, 256, 65536, stream>>>(x, ws);
  if (fused) {
    kf_ffn<<<256, 384, KF_LDS, stream>>>(ws, (uint16_t*)d_out);
  } else {
    k4_ffn_up<<<256, 256, K4_LDS, stream>>>(ws);
    k5_ffn_down<<<224, 256, 65536, stream>>>(ws, (uint16_t*)d_out);
  }
}
```
